```python
import torch

def _softmax(x, axis=-1):
    return torch.softmax(x, dim=axis)
def _gelu(x):
    return torch.nn.functional.gelu(x, approximate="tanh")
def _silu(x):
    return torch.nn.functional.silu(x)
def _sigmoid(x):
    return torch.sigmoid(x)
def _layernorm(x, axis=-1, eps=1e-5):
    m = x.mean(dim=axis, keepdim=True)
    v = x.var(dim=axis, keepdim=True, unbiased=False)
    return (x - m) / torch.sqrt(v + eps)
def _scatter_add(tgt, idx, src, axis=0):
    return tgt.clone().index_add_(axis, idx, src)
def _segment_sum(src, seg, n):
    out = torch.zeros((n,) + src.shape[1:], dtype=src.dtype)
    return out.index_add_(0, seg, src)
def _pad(x, pw):
    flat = tuple(v for pair in reversed(pw) for v in pair)
    return torch.nn.functional.pad(x, flat)

kernel_name = 'parallel_sum__linear_kernel_hedgehog__dense_activation_mish__prompt'

def setup_inputs():

    _w = (lambda a: (lambda t: t.to(torch.bfloat16) if t.is_floating_point() else t)(torch.as_tensor(a)))
    def _A_setup():
        B, S, D, H = 1, 2048, 448, 16
        Dh = D // H
        return {
            "x": _w((lambda *s: torch.randn(*s, dtype=torch.bfloat16) * 0.05)(B, S, D)),
            "wqkv": _w((lambda *s: torch.randn(*s, dtype=torch.bfloat16) * 0.2)(D, 3 * D)),
            "w_phi": _w((lambda *s: torch.randn(*s, dtype=torch.bfloat16) * 0.2)(H, Dh, Dh) * (Dh ** -0.5)),
            "wo": _w((lambda *s: torch.randn(*s, dtype=torch.bfloat16) * 0.05)(D, D)),
        }
    _A_in = _A_setup()

    def _B_setup():
        B, S, D, F = 1, 2048, 448, 1792
        return {
            "x": _w((lambda *s: torch.randn(*s, dtype=torch.bfloat16) * 0.05)(B, S, D)),
            "w1": _w((lambda *s: torch.randn(*s, dtype=torch.bfloat16) * 0.8)(D, F) * (D ** -0.5)),
            "b1": _w((lambda *s: torch.randn(*s, dtype=torch.bfloat16) * 0.4)(F)),
            "w2": _w((lambda *s: torch.randn(*s, dtype=torch.bfloat16) * 0.1)(F, D) * (F ** -0.5)),
        }
    _B_in = _B_setup()

    return {
        "A_x": _A_in["x"],
        "A_wqkv": _A_in["wqkv"],
        "A_w_phi": _A_in["w_phi"],
        "A_wo": _A_in["wo"],
        "B_x": _B_in["x"],
        "B_w1": _B_in["w1"],
        "B_b1": _B_in["b1"],
        "B_w2": _B_in["w2"],
    }

def reference(A_x, A_wqkv, A_w_phi, A_wo, B_x, B_w1, B_b1, B_w2):
    def _A_ref(x, wqkv, w_phi, wo):
        B, S, D = x.shape
        H = 16; Dh = D // H
        qkv = (lambda x, *d: x.permute(*d))((x @ wqkv).reshape(B, S, 3, H, Dh), 2, 0, 3, 1, 4)
        q, k, v = qkv[0], qkv[1], qkv[2]
        pq = torch.einsum("bhsd,hdf->bhsf", q, w_phi)
        pk = torch.einsum("bhsd,hdf->bhsf", k, w_phi)
        phi_q = torch.concatenate([_softmax(pq, axis=-1), _softmax(-pq, axis=-1)], axis=-1)
        phi_k = torch.concatenate([_softmax(pk, axis=-1), _softmax(-pk, axis=-1)], axis=-1)
        kv = torch.einsum("bhsf,bhsd->bhfd", phi_k, v)
        z  = torch.einsum("bhsf->bhf", phi_k)
        num = torch.einsum("bhqf,bhfd->bhqd", phi_q, kv)
        den = torch.einsum("bhqf,bhf->bhq", phi_q, z)[..., None] + 1e-6
        o = num / den
        return (lambda x, *d: x.permute(*d))(o, 0, 2, 1, 3).reshape(B, S, D) @ wo

    def _B_ref(x, w1, b1, w2):
        h = torch.einsum("bsd,df->bsf", x, w1) + b1
        sp = torch.log1p(torch.exp(-torch.abs(h))) + torch.where(h > 0, h, torch.zeros_like(h))
        act = h * torch.tanh(sp)
        return torch.einsum("bsf,fd->bsd", act, w2)

    x = A_x
    xn = _layernorm(x, axis=-1)
    return x + _A_ref(xn, A_wqkv, A_w_phi, A_wo) + _B_ref(xn, B_w1, B_b1, B_w2)
```

```cpp
#include <hip/hip_runtime.h>
#include <hip/hip_bf16.h>
typedef unsigned short u16;
constexpr int S=2048, D=448, H=16, DH=28, F=1792;
__device__ __forceinline__ float b2f(u16 v){ return __uint_as_float(((unsigned)v)<<16); }
__device__ __forceinline__ u16 f2b(float x){ unsigned u=__float_as_uint(x); u += 0x7fffu + ((u>>16)&1u); return (u16)(u>>16); }

__global__ void k_ln(const u16* x, float* xn){
  int r=blockIdx.x; int t=threadIdx.x;
  float v[7]; float s=0;
  for(int i=0;i<7;i++){ v[i]=b2f(x[r*D+t+64*i]); s+=v[i]; }
  for(int o=32;o;o>>=1) s+=__shfl_xor(s,o);
  float m=s/D; float q=0;
  for(int i=0;i<7;i++){ float d=v[i]-m; q+=d*d; }
  for(int o=32;o;o>>=1) q+=__shfl_xor(q,o);
  float rs=1.f/sqrtf(q/D+1e-5f);
  for(int i=0;i<7;i++) xn[r*D+t+64*i]=(v[i]-m)*rs;
}
__global__ void k_gemm(const float* A, const u16* B, float* C, int M,int N,int K){
  int n=blockIdx.x*64+threadIdx.x; int m0=blockIdx.y*8;
  if(n>=N) return;
  float acc[8]={0,0,0,0,0,0,0,0};
  for(int k=0;k<K;k++){ float b=b2f(B[k*N+n]);
    #pragma unroll
    for(int i=0;i<8;i++) acc[i]+=A[(m0+i)*K+k]*b; }
  for(int i=0;i<8;i++) C[(m0+i)*N+n]=acc[i];
}
__global__ void k_mish(float* h, const u16* b1){
  int i=blockIdx.x*256+threadIdx.x; int f=i%F;
  float v=h[i]+b2f(b1[f]);
  float u=__expf(fminf(v,30.f)); float n=u*(u+2.f);
  h[i]=v*n/(n+2.f);
}
__global__ void k_phi(const float* qkv, const u16* wphi, float* phi){
  int idx=blockIdx.x*64+threadIdx.x;
  int h=idx%H; int s=(idx/H)%S; int w=idx/(H*S);
  const float* q=qkv+s*(3*D)+w*D+h*DH;
  float* o=phi+(size_t)idx*56;
  float mx=-1e30f, mn=1e30f;
  for(int f=0;f<DH;f++){ float a=0; for(int d=0;d<DH;d++) a+=q[d]*b2f(wphi[h*DH*DH+d*DH+f]); o[f]=a; mx=fmaxf(mx,a); mn=fminf(mn,a);}
  float s1=0,s2=0;
  for(int f=0;f<DH;f++){ float a=o[f]; float e1=__expf(a-mx), e2=__expf(mn-a); o[f]=e1; o[DH+f]=e2; s1+=e1; s2+=e2;}
  for(int f=0;f<DH;f++){ o[f]/=s1; o[DH+f]/=s2; }
}
__global__ void k_kv(const float* qkv, const float* phi, float* kv){
  int h=blockIdx.x; int f=blockIdx.y; int d=threadIdx.x;
  if(d>28) return;
  const float* pk=phi+(size_t)S*H*56;
  float a=0;
  for(int s=0;s<S;s++){ float p=pk[((size_t)s*H+h)*56+f]; float v= d<28? qkv[s*(3*D)+2*D+h*DH+d]:1.f; a+=p*v; }
  kv[(h*56+f)*29+d]=a;
}
__global__ void k_o(const float* phi, const float* kv, float* o){
  int idx=blockIdx.x*64+threadIdx.x;
  int s=idx/D, c=idx%D, h=c/DH, d=c%DH;
  const float* pq=phi+((size_t)s*H+h)*56;
  float num=0, den=0;
  for(int f=0;f<56;f++){ num+=pq[f]*kv[(h*56+f)*29+d]; den+=pq[f]*kv[(h*56+f)*29+28]; }
  o[idx]=num/(den+1e-6f);
}
__global__ void k_fin(const u16* x, const float* a, const float* b, u16* out){
  int i=blockIdx.x*256+threadIdx.x;
  out[i]=f2b(b2f(x[i])+a[i]+b[i]);
}
extern "C" void kernel_launch(void* const* d_in, const int* in_sizes, int n_in, void* d_out, int out_size, void* d_ws, size_t ws_size, hipStream_t stream){
  const u16* x=(const u16*)d_in[0]; const u16* wqkv=(const u16*)d_in[1]; const u16* wphi=(const u16*)d_in[2]; const u16* wo=(const u16*)d_in[3];
  const u16* w1=(const u16*)d_in[5]; const u16* b1=(const u16*)d_in[6]; const u16* w2=(const u16*)d_in[7];
  float* ws=(float*)d_ws;
  float* xn=ws; float* qkv=xn+S*D; float* h=qkv+S*3*D; float* phi=h+S*F; float* kv=phi+2*S*H*56; float* o=kv+H*56*29+64; float* a=o+S*D; float* b=a+S*D;
  k_ln<<<S,64,0,stream>>>(x,xn);
  k_gemm<<<dim3(21,S/8),64,0,stream>>>(xn,wqkv,qkv,S,3*D,D);
  k_gemm<<<dim3(28,S/8),64,0,stream>>>(xn,w1,h,S,F,D);
  k_mish<<<S*F/256,256,0,stream>>>(h,b1);
  k_phi<<<2*S*H/64,64,0,stream>>>(qkv,wphi,phi);
  k_kv<<<dim3(H,56),32,0,stream>>>(qkv,phi,kv);
  k_o<<<S*D/64,64,0,stream>>>(phi,kv,o);
  k_gemm<<<dim3(7,S/8),64,0,stream>>>(o,wo,a,S,D,D);
  k_gemm<<<dim3(7,S/8),64,0,stream>>>(h,w2,b,S,D,F);
  k_fin<<<S*D/256,256,0,stream>>>(x,a,b,(u16*)d_out);
}
```

```cpp
#include <hip/hip_runtime.h>
#include <stdint.h>
#include <type_traits>

typedef unsigned short u16;
typedef unsigned long long u64;
typedef __bf16 bf16x2 __attribute__((ext_vector_type(2)));
typedef __bf16 bf16x4 __attribute__((ext_vector_type(4)));
typedef __bf16 bf16x8 __attribute__((ext_vector_type(8)));
typedef float f32x2 __attribute__((ext_vector_type(2)));
typedef float f32x4 __attribute__((ext_vector_type(4)));
typedef float f32x16 __attribute__((ext_vector_type(16)));
typedef unsigned u32x2 __attribute__((ext_vector_type(2)));
typedef unsigned u32x4 __attribute__((ext_vector_type(4)));
#define LDS3 __attribute__((address_space(3)))
#define DEV __device__ __forceinline__

constexpr unsigned WS_PF = 1024, WS_AF = WS_PF + 2048, WS_KF = WS_AF + 2048, WS_OF = WS_KF + 2048;
constexpr unsigned WS_WP = 16384, WP_HEAD = 86016, WP_Q = 57344;
constexpr unsigned WS_KVP = WS_WP + 16 * WP_HEAD;
constexpr unsigned WS_ACT = WS_KVP + 16 * 4 * 16 * 1024;
constexpr unsigned WS_END = WS_ACT + 64 * 144 * 1024;
constexpr unsigned WS_DBG = WS_END;
constexpr unsigned SLOT = 16384;
constexpr unsigned L_PHI = 131072, L_VFR = L_PHI + 16384, L_ST = L_VFR + 8192, L_CS = L_ST + 2048;
constexpr unsigned LDS_BYTES = 163840;
#define LA 3
#define KP 16
#define OP 26
#define SPIN_MAX 2000000

template <int I, int N, class F> DEV void sfor(F&& f) {
  if constexpr (I < N) { f(std::integral_constant<int, I>{}); sfor<I + 1, N>(f); }
}
constexpr int cmin(int a, int b) { return a < b ? a : b; }
constexpr int cmax(int a, int b) { return a > b ? a : b; }

template <int ROLE, int CH> struct Led {
  static constexpr int pp = ROLE == 0 ? 8 : 4;
  static constexpr int nb1 = (ROLE == 0 && CH == 0) ? 2 : 0;
  static constexpr int pre = CH == 0 ? nb1 + 4 * pp : 0;
  static constexpr int G(int k) {
    if (CH != 0) return 0;
    if (k >= 1 && k <= 3) return pp;
    if (k == 5) return 8;
    if (k == 6) return 6;
    if (k == 7) return 7;
    if (ROLE != 0) return 0;
    if (k == 4) return 8;
    if (k >= 8 && k <= 13) return 4;
    if (k >= 15 && k <= 23) return 4;
    if (k == 24) return 2;
    return 0;
  }
  static constexpr int LX(int k) {
    if (ROLE != 0) return 0;
    return (k == KP ? 8 : 0) + (k == OP ? 9 : 0);
  }
  static constexpr int LAct(int k) {
    if (ROLE != 0) return 0;
    if (k == 13) return 2 * LA;
    if (k >= 14 && k - 14 + LA <= 13) return 2;
    return 0;
  }
  static constexpr int St(int k) {
    if (ROLE != 0) return 0;
    if (k == 7 || k == 8) return 2;
    if (CH == 1 && k == KP + 3) return 2;
    return 0;
  }
  static constexpr int cum(int k) { int s = pre; for (int i = 0; i < k; i++) s += G(i) + LX(i) + LAct(i) + St(i); return s; }
  static constexpr int need(int k) {
    if (CH == 0) {
      if (k <= 3) return nb1 + (k + 1) * pp;
      if (k <= 6) return cum(k - 3) + pp;
      if (k == 7) return cum(5) + 4;
      if (k == 8) return cum(5) + 8;
      if (k == 9) return cum(6) + 4;
      if (k == 10) return cum(6) + 6;
      if (k == 11) return ROLE == 0 ? cum(9) : cum(7) + 4;
      if (k == 12) return ROLE == 0 ? cum(9) : cum(7) + 7;
      if (ROLE != 0) return -1;
      if (k >= 16 && k <= 21) return cum(k - 8) + 4;
      if (k >= 22 && k <= 27) return cum(k - 7) + 4;
      if (k == 28) return cum(24) + 2;
      return -1;
    } else {
      if (k == 2 || k == 11 || k == 13) return cum(k);
      if (ROLE == 0 && k == KP + 5) return cum(KP + 4);
      return -1;
    }
  }
  static constexpr int barN(int k) { int n = need(k); return n < 0 ? 63 : cmin(63, cum(k) - n); }
  static constexpr int mid(int k) { return cum(k) + G(k) + LX(k) + LAct(k); }
  static constexpr int actEnd(int u) { return u < LA ? cum(13) + G(13) + LX(13) + 2 * (u + 1) : mid(14 + u - LA); }
  static constexpr int actN(int u) { return cmin(63, mid(14 + u) - actEnd(u)); }
  static constexpr int t4N() { return cmin(63, mid(KP + 2) - (cum(KP) + G(KP) + 8)); }
  static constexpr int oN() { return cmin(63, cum(28) - (cum(OP) + G(OP) + LX(OP))); }
};

DEV u32x4 mksrd(const void* p, unsigned bytes) {
  u64 a = (u64)p;
  u32x4 s; s[0] = (unsigned)a; s[1] = (unsigned)(a >> 32) & 0xffffu; s[2] = bytes; s[3] = 0x00020000u;
  return s;
}
template <bool SC1> DEV void glds(unsigned m0, unsigned voff, u32x4 srd, unsigned soff) {
  if constexpr (SC1)
    asm volatile("s_mov_b32 m0, %0\n\ts_nop 0\n\tbuffer_load_dwordx4 %1, %2, %3 offen sc1 lds" ::"s"(m0), "v"(voff), "s"(srd), "s"(soff) : "memory");
  else
    asm volatile("s_mov_b32 m0, %0\n\ts_nop 0\n\tbuffer_load_dwordx4 %1, %2, %3 offen lds" ::"s"(m0), "v"(voff), "s"(srd), "s"(soff) : "memory");
}
template <bool SC1, int OFF> DEV void vload(u32x4& d, unsigned voff, u32x4 srd, unsigned soff) {
  if constexpr (SC1)
    asm volatile("buffer_load_dwordx4 %0, %1, %2, %3 offen offset:%4 sc1" : "=v"(d) : "v"(voff), "s"(srd), "s"(soff), "n"(OFF) : "memory");
  else
    asm volatile("buffer_load_dwordx4 %0, %1, %2, %3 offen offset:%4" : "=v"(d) : "v"(voff), "s"(srd), "s"(soff), "n"(OFF) : "memory");
}
template <int OFF> DEV void vstore(u32x4 d, unsigned voff, u32x4 srd, unsigned soff) {
  asm volatile("buffer_store_dwordx4 %0, %1, %2, %3 offen offset:%4 sc1\n\ts_nop 1" ::"v"(d), "v"(voff), "s"(srd), "s"(soff), "n"(OFF) : "memory");
}
template <int N> DEV void bar() { asm volatile("s_waitcnt vmcnt(%0) lgkmcnt(0)\n\ts_barrier" ::"n"(N) : "memory"); }

DEV f32x16 mfma(u32x4 a, u32x4 b, f32x16 c) {
  return __builtin_amdgcn_mfma_f32_32x32x16_bf16(__builtin_bit_cast(bf16x8, a), __builtin_bit_cast(bf16x8, b), c, 0, 0, 0);
}
DEV float dot2(unsigned a, unsigned b, float c) {
  return __builtin_amdgcn_fdot2_f32_bf16(__builtin_bit_cast(bf16x2, a), __builtin_bit_cast(bf16x2, b), c, false);
}
DEV unsigned pk2(float lo, float hi) { f32x2 v = {lo, hi}; return __builtin_bit_cast(unsigned, __builtin_convertvector(v, bf16x2)); }
DEV float bflo(unsigned v) { return __uint_as_float(v << 16); }
DEV float bfhi(unsigned v) { return __uint_as_float(v & 0xffff0000u); }
DEV u32x4 ld128(LDS3 char* p) { return *(LDS3 u32x4*)p; }
DEV u32x4 ldtr(LDS3 char* p0, LDS3 char* p1) {
  bf16x4 a = __builtin_amdgcn_ds_read_tr16_b64_v4bf16((LDS3 bf16x4*)p0);
  bf16x4 b = __builtin_amdgcn_ds_read_tr16_b64_v4bf16((LDS3 bf16x4*)p1);
  return __builtin_bit_cast(u32x4, __builtin_shufflevector(a, b, 0, 1, 2, 3, 4, 5, 6, 7));
}
DEV u32x4 pack8(const f32x16& a, int o) {
  u32x4 r; r[0] = pk2(a[o], a[o + 1]); r[1] = pk2(a[o + 2], a[o + 3]); r[2] = pk2(a[o + 4], a[o + 5]); r[3] = pk2(a[o + 6], a[o + 7]);
  return r;
}
DEV float xhalf_max(float v) { auto s = __builtin_amdgcn_permlane32_swap(__float_as_uint(v), __float_as_uint(v), false, false); return fmaxf(__uint_as_float(s[0]), __uint_as_float(s[1])); }
DEV float xhalf_min(float v) { auto s = __builtin_amdgcn_permlane32_swap(__float_as_uint(v), __float_as_uint(v), false, false); return fminf(__uint_as_float(s[0]), __uint_as_float(s[1])); }
DEV float xhalf_add(float v) { auto s = __builtin_amdgcn_permlane32_swap(__float_as_uint(v), __float_as_uint(v), false, false); return __uint_as_float(s[0]) + __uint_as_float(s[1]); }
DEV u32x4 cfrag(float cs, unsigned e16) {
  unsigned hi = pk2(cs, cs);
  float lo = cs - bflo(hi);
  u32x4 r; r[0] = hi; r[1] = (pk2(lo, 0.f) & 0xffffu) | (e16 << 16); r[2] = e16; r[3] = 0;
  return r;
}
DEV float sum4(f32x4 v) { return (v[0] + v[1]) + (v[2] + v[3]); }

DEV void dual_softmax(const f32x16& a, float rl, float sc, bool hi_half, f32x16& p, f32x16& n) {
  float t[16];
#pragma unroll
  for (int v = 0; v < 16; v++) t[v] = (v >= 12 && hi_half) ? a[0] : a[v];
  float mx = t[0], mn = t[0];
#pragma unroll
  for (int v = 1; v < 16; v++) { mx = fmaxf(mx, t[v]); mn = fminf(mn, t[v]); }
  mx = xhalf_max(mx); mn = xhalf_min(mn);
  float c1 = -mx * rl, c2 = mn * rl, s1 = 0.f, s2 = 0.f;
#pragma unroll
  for (int v = 0; v < 16; v++) {
    float e1 = __builtin_amdgcn_exp2f(__builtin_fmaf(t[v], rl, c1));
    float e2 = __builtin_amdgcn_exp2f(__builtin_fmaf(-t[v], rl, c2));
    if (v >= 12) { e1 = hi_half ? 0.f : e1; e2 = hi_half ? 0.f : e2; }
    p[v] = e1; n[v] = e2; s1 += e1; s2 += e2;
  }
  s1 = sc * __builtin_amdgcn_rcpf(xhalf_add(s1));
  s2 = sc * __builtin_amdgcn_rcpf(xhalf_add(s2));
#pragma unroll
  for (int v = 0; v < 16; v++) { p[v] *= s1; n[v] *= s2; }
}

DEV void poll(const char* ws, unsigned off, u64 tag) {
  const u64* p = (const u64*)(ws + off);
#pragma unroll 1
  for (int it = 0; it < SPIN_MAX; it++) {
    const u64 v = __hip_atomic_load(p, __ATOMIC_RELAXED, __HIP_MEMORY_SCOPE_AGENT);
    if (__builtin_amdgcn_ballot_w64(v != tag) == 0) break;
    __builtin_amdgcn_s_sleep(1);
  }
}
DEV void flag(char* ws, unsigned off, u64 tag, int l) {
  if (l == 0) __hip_atomic_store((u64*)(ws + off), tag, __ATOMIC_RELAXED, __HIP_MEMORY_SCOPE_AGENT);
}

#define TS(k) do {} while (0)

template <int ROLE, int CH>
DEV void body(const u16* __restrict__ x, const u16* __restrict__ wqkv, const u16* __restrict__ wphi, const u16* __restrict__ wo,
              const u16* __restrict__ w1, const u16* __restrict__ b1, const u16* __restrict__ w2, u16* __restrict__ out, char* ws,
              LDS3 char* lds, const int w, const int r, const int c, const int R, const int nt) {
  using L = Led<ROLE, CH>;
  const int l = threadIdx.x & 63, lr = l & 31, hh = l >> 5;
  const int g = w & 3;
  const int q4 = (l & 15) >> 2, p4 = l & 3, blk = (l >> 4) & 1;
  const unsigned ldsb = (unsigned)(size_t)lds;
  TS(40);
  const u32x4 sX = mksrd(x, 2048 * 896), sW1 = mksrd(w1, 448 * 3584), sW2 = mksrd(w2, 1792 * 896), sWO = mksrd(wo, 448 * 896);
  const u32x4 sWS = mksrd(ws, WS_END);
  const unsigned m0w = ldsb + g * 1024;
  const unsigned vX = (8 * g + (l >> 3)) * 896 + (((l & 7) ^ (((g & 1) << 2) | (l >> 4))) << 4);
  const unsigned vW1 = (4 * g + (l >> 4)) * 3584 + (((l & 15) ^ ((l >> 4) << 2)) << 4);
  const unsigned vW2 = (8 * g + (l >> 3)) * 896 + (((l & 7) ^ (((l >> 4) & 1) << 2)) << 4);
  const unsigned vLin = g * 1024 + l * 16;
  const unsigned l16 = l * 16;

  auto pageX = [&](int kc, int slot) {
#pragma unroll
    for (int j = 0; j < 4; j++) glds<false>(m0w + slot * SLOT + j * 4096, vX, sX, (128 * r + 32 * j) * 896 + kc * 128);
  };
  auto pageW1 = [&](int kc, int slot) {
#pragma unroll
    for (int j = 0; j < 4; j++) glds<false>(m0w + slot * SLOT + j * 4096, vW1, sW1, (64 * kc + 16 * j) * 3584 + c * 256);
  };
  auto pageW2 = [&](int u, int slot) {
#pragma unroll
    for (int j = 0; j < 4; j++) glds<false>(m0w + slot * SLOT + j * 4096, vW2, sW2, (128 * u + 32 * j) * 896 + nt * 128);
  };
  auto pageWO = [&](int u, int n) {
#pragma unroll
    for (int j = 0; j < n; j++) glds<false>(m0w + (4 + u) * SLOT + j * 4096, vW2, sWO, (128 * u + 32 * j) * 896 + nt * 128);
  };
  auto pageLin = [&](unsigned off, int slot, int n) {
#pragma unroll
    for (int j = 0; j < n; j++) glds<true>(m0w + slot * SLOT + j * 4096, vLin, sWS, WS_WP + c * WP_HEAD + off + j * 4096);
  };

  unsigned b1v[2] = {0, 0};
  u64 tag = 0;
  u32x4 pA0 = {0, 0, 0, 0}, pA1 = {0, 0, 0, 0}, pB0 = {0, 0, 0, 0}, pB1 = {0, 0, 0, 0};
  if constexpr (CH == 0) {
    if constexpr (ROLE == 0) {
      const u32x4 sB1 = mksrd(b1, 1792 * 2);
      const unsigned vb = (128 * c + lr) * 2;
      asm volatile("buffer_load_ushort %0, %1, %2, 0 offen" : "=v"(b1v[0]) : "v"(vb), "s"(sB1) : "memory");
      asm volatile("buffer_load_ushort %0, %1, %2, 0 offen offset:64" : "=v"(b1v[1]) : "v"(vb), "s"(sB1) : "memory");
    }
#pragma unroll
    for (int kc = 0; kc < 4; kc++) { pageX(kc, 2 * kc); if constexpr (ROLE == 0) pageW1(kc, 2 * kc + 1); }
  } else {
    if (w == 7) tag = __hip_atomic_load((const u64*)ws, __ATOMIC_RELAXED, __HIP_MEMORY_SCOPE_AGENT) + 1;
    if constexpr (ROLE == 0) {
      b1v[0] = b1[128 * c + 64 + lr]; b1v[1] = b1[128 * c + 96 + lr];
      if (w != 7) {
        const int kk = (lr & 0x13) | ((lr & 4) << 1) | ((lr & 8) >> 1);
        const char* a = (const char*)wqkv + (32 * c + kk) * 2688 + (g * 448 + 28 * r + 8 * hh) * 2;
        u32x2 a0 = *(const u32x2*)a, a1 = *(const u32x2*)(a + 8), a2 = *(const u32x2*)(a + 32), a3 = *(const u32x2*)(a + (hh ? 32 : 40));
        pA0 = u32x4{a0[0], a0[1], a1[0], a1[1]}; pA1 = u32x4{a2[0], a2[1], a3[0], a3[1]};
        if (g < 2) {
          __amdgpu_buffer_rsrc_t rp = __builtin_amdgcn_make_buffer_rsrc((void*)wphi, 0, 16 * 784 * 2, 0x00020000);
          unsigned e[16];
#pragma unroll
          for (int i = 0; i < 16; i++) {
            const int d = (i >> 3) * 16 + 8 * hh + (i & 7);
            const unsigned o = (d < 28 && lr < 28) ? (unsigned)(r * 784 + d * 28 + lr) * 2u : 0x40000000u;
            e[i] = __builtin_amdgcn_raw_buffer_load_b16(rp, o, 0, 0);
          }
#pragma unroll
          for (int i = 0; i < 4; i++) { pB0[i] = e[2 * i] | (e[2 * i + 1] << 16); pB1[i] = e[8 + 2 * i] | (e[9 + 2 * i] << 16); }
        } else {
#pragma unroll
          for (int i = 0; i < 4; i++) {
            const int d = 8 * hh + 2 * i;
            pB0[i] = (lr == d ? 0x3F80u : 0u) | (lr == d + 1 ? 0x3F800000u : 0u);
            pB1[i] = lr < 28 ? ((lr == d + 16 ? 0x3F80u : 0u) | (lr == d + 17 ? 0x3F800000u : 0u)) : 0u;
          }
        }
      }
    }
  }

  unsigned xa[4], wa[4][2], rot[4];
#pragma unroll
  for (int tt = 0; tt < 4; tt++) {
    const int s = (tt + g) & 3, row = 32 * g + lr;
    xa[tt] = row * 128 + (((2 * s + hh) ^ ((row >> 1) & 7)) << 4);
#pragma unroll
    for (int i = 0; i < 2; i++) wa[tt][i] = (16 * s + 8 * hh + q4) * 256 + ((((8 * CH + 4 * i) ^ (q4 << 2)) + 2 * blk + (p4 >> 1)) << 4) + 8 * (p4 & 1);
    rot[tt] = s;
  }

  u32x4 xf[28];
  f32x16 acc[2] = {{0}, {0}};
  float cs[2] = {0.f, 0.f}, st0 = 0.f, st1 = 0.f;
  const unsigned ONES = 0x3F803F80u;

  sfor<0, 7>([&](auto KC) {
    constexpr int kc = KC;
    if constexpr (CH == 1 && kc == 5) { if (w == 7) poll(ws, WS_PF + c * 128 + (l < 14 ? l : 0) * 8, tag); }
    bar<L::barN(kc)>();
    TS(kc);
    if constexpr (CH == 0 && ROLE == 0 && kc == 0) asm volatile("" : "+v"(b1v[0]), "+v"(b1v[1]));
    if constexpr (CH == 1 && ROLE == 0 && kc == 2) { if (w == 7) flag(ws, WS_PF + r * 128 + c * 8, tag, l); }
    constexpr unsigned sx = ((2 * kc) & 7) * SLOT, sw = sx + SLOT;
    u32x4 af[4][2];
    sfor<0, 4>([&](auto TT) {
      constexpr int tt = TT;
      xf[4 * kc + tt] = ld128(lds + xa[tt] + sx);
      if constexpr (ROLE == 0) {
        af[tt][0] = ldtr(lds + wa[tt][0] + sw, lds + wa[tt][0] + sw + 1024);
        af[tt][1] = ldtr(lds + wa[tt][1] + sw, lds + wa[tt][1] + sw + 1024);
      }
      if constexpr (tt == 0) {
        if constexpr (CH == 0) {
          if constexpr (kc >= 1 && kc <= 3) { pageX(kc + 3, (2 * kc + 6) & 7); if constexpr (ROLE == 0) pageW1(kc + 3, (2 * kc + 7) & 7); }
          if constexpr (kc == 4 && ROLE == 0) { pageW2(0, 6); pageW2(1, 7); }
          if constexpr (kc == 5) { pageLin(0, 0, 4); pageLin(16384, 1, 4); }
          if constexpr (kc == 6) { pageLin(32768, 2, 4); pageLin(49152, 3, 2); }
        } else if constexpr (ROLE == 0 && kc == 0) {
          asm volatile("" : "+v"(b1v[0]), "+v"(b1v[1]));
          if (w != 7) {
            f32x16 d = {0};
            d = mfma(pA0, pB0, d); d = mfma(pA1, pB1, d);
            const unsigned so = WS_WP + r * WP_HEAD + (g == 0 ? WP_Q + 2 * c * 1024 : 2 * c * 2048 + (g - 1) * 1024);
            vstore<0>(pack8(d, 0), l16, sWS, so);
            vstore<0>(pack8(d, 8), l16, sWS, so + (g == 0 ? 1024 : 2048));
          }
        }
      }
      const u32x4 xv = xf[4 * kc + tt];
      if constexpr (ROLE == 0) {
        acc[0] = mfma(af[tt][0], xv, acc[0]);
        acc[1] = mfma(af[tt][1], xv, acc[1]);
        if constexpr (tt == 0) {
#pragma unroll
          for (int i = 0; i < 4; i++) { cs[0] = dot2(af[0][0][i], ONES, cs[0]); cs[1] = dot2(af[0][1][i], ONES, cs[1]); }
        }
      }
      st0 = dot2(xv[0], CH == 0 ? ONES : xv[0], st0); st1 = dot2(xv[1], CH == 0 ? ONES : xv[1], st1);
      st0 = dot2(xv[2], CH == 0 ? ONES : xv[2], st0); st1 = dot2(xv[3], CH == 0 ? ONES : xv[3], st1);
    });
  });
  *(LDS3 float*)(lds + L_ST + CH * 1024 + (32 * g + lr) * 8 + hh * 4) = st0 + st1;
  if constexpr (ROLE == 0) {
    *(LDS3 float*)(lds + L_CS + (2 * CH) * 1024 + l16 + g * 4) = cs[0];
    *(LDS3 float*)(lds + L_CS + (2 * CH + 1) * 1024 + l16 + g * 4) = cs[1];
  }

  u32x4 mfrag;
  float rr, rl;
  f32x16 accB = {0};
  float csB = 0.f;
  const unsigned actv = (4 * r + g) * 144 * 1024 + l16;
  auto mish8 = [&](const f32x16& a, int o) {
    float t[8];
#pragma unroll
    for (int j = 0; j < 8; j++) {
      const float u = __builtin_amdgcn_exp2f(a[o + j] * rl);
      const float d = __builtin_fmaf(u, u + 2.f, 2.f);
      const float k = __builtin_fmaf(-2.f, __builtin_amdgcn_rcpf(d), 1.f);
      t[j] = (a[o + j] * rr) * k;
    }
    return u32x4{pk2(t[0], t[1]), pk2(t[2], t[3]), pk2(t[4], t[5]), pk2(t[6], t[7])};
  };
  sfor<0, 4>([&](auto V) {
    constexpr int v = V, k = 7 + v;
    bar<L::barN(k)>();
    TS(k);
    if constexpr (v == 0) {
      const f32x2 s1 = *(LDS3 f32x2*)(lds + L_ST + (32 * g + lr) * 8), s2 = *(LDS3 f32x2*)(lds + L_ST + 1024 + (32 * g + lr) * 8);
      const float m = (s1[0] + s1[1]) * (1.f / 448.f);
      const float var = fmaxf((s2[0] + s2[1]) * (1.f / 448.f) - m * m, 0.f) + 1e-5f;
      rr = __builtin_amdgcn_rsqf(var);
      const float sg = var * rr;
      rl = rr * 1.44269504088896f;
      const unsigned mh = pk2(-m, sg);
      const unsigned ml = pk2(-m - bflo(mh), sg - bfhi(mh));
      mfrag[0] = (mh & 0xffffu) | (ml << 16); mfrag[1] = mh; mfrag[2] = ml >> 16; mfrag[3] = 0;
      if constexpr (ROLE == 0) {
#pragma unroll
        for (int i = 0; i < 2; i++) {
          const float t = sum4(*(LDS3 f32x4*)(lds + L_CS + (2 * CH + i) * 1024 + l16));
          acc[i] = mfma(cfrag(t, hh ? 0u : b1v[i]), mfrag, acc[i]);
        }
      }
    }
    sfor<0, (v == 3 ? 4 : 8)>([&](auto S) {
      constexpr int s = S, kc = 2 * v + (s >> 2), tt = s & 3;
      const u32x4 f = ld128(lds + rot[tt] * 2048 + l16 + (CH * 1024 + v * SLOT + (kc & 1) * 8192));
      if constexpr (s == 0 && CH == 0) {
        if constexpr (v == 0) { pageLin(WP_Q, 4, 4); pageLin(WP_Q + 16384, 5, 3); }
        else if constexpr (ROLE == 0) pageW2(1 + v, v - 1);
      }
      if constexpr (CH == 0) accB = mfma(f, xf[4 * kc + tt], accB); else accB = mfma(xf[4 * kc + tt], f, accB);
      if constexpr (tt == 0) {
#pragma unroll
        for (int i = 0; i < 4; i++) csB = dot2(f[i], ONES, csB);
      }
    });
    if constexpr (ROLE == 0 && v < 2) {
      const unsigned so = WS_ACT + (8 * c + 4 * CH + 2 * v) * 1024;
      vstore<0>(mish8(acc[v], 0), actv, sWS, so);
      vstore<1024>(mish8(acc[v], 8), actv, sWS, so);
    }
  });
  *(LDS3 float*)(lds + L_CS + CH * 1024 + l16 + g * 4) = csB;

  bar<L::barN(11)>();
  TS(11);
  f32x16 accQ = {0};
  float csQ = 0.f;
  u32x4 phq[4];
  if constexpr (CH == 0) {
    const float t = sum4(*(LDS3 f32x4*)(lds + L_CS + l16));
    if constexpr (ROLE == 0) pageW2(5, 3);
    accB = mfma(cfrag(t, 0u), mfrag, accB);
    f32x16 p, n;
    dual_softmax(accB, rl, rr, hh != 0, p, n);
    const int T = 32 * g + lr;
    const unsigned gs = (T & 5) | ((T & 2) << 2) | ((T & 8) >> 2);
#pragma unroll
    for (int u = 0; u < 4; u++) {
      *(LDS3 u32x2*)(lds + L_PHI + T * 128 + (((2 * u + hh) ^ gs) << 3)) = u32x2{pk2(p[4 * u], p[4 * u + 1]), pk2(p[4 * u + 2], p[4 * u + 3])};
      *(LDS3 u32x2*)(lds + L_PHI + T * 128 + (((8 + 2 * u + hh) ^ gs) << 3)) = u32x2{pk2(n[4 * u], n[4 * u + 1]), pk2(n[4 * u + 2], n[4 * u + 3])};
    }
  } else {
    if constexpr (ROLE == 0) { if (w == 7) flag(ws, WS_AF + r * 128 + c * 8, tag, l); }
    const float t = sum4(*(LDS3 f32x4*)(lds + L_CS + 1024 + l16));
    accB = mfma(mfrag, cfrag(t, l == 28 ? 0x3F80u : 0u), accB);
    *(LDS3 u32x4*)(lds + L_VFR + 2 * g * 1024 + l16) = pack8(accB, 0);
    *(LDS3 u32x4*)(lds + L_VFR + (2 * g + 1) * 1024 + l16) = pack8(accB, 8);
    sfor<0, 16>([&](auto S) {
      constexpr int s = S, kc = s >> 2, tt = s & 3;
      const u32x4 f = ld128(lds + rot[tt] * 1024 + l16 + (4 * SLOT + kc * 4096));
      accQ = mfma(f, xf[4 * kc + tt], accQ);
      if constexpr (tt == 0) {
#pragma unroll
        for (int i = 0; i < 4; i++) csQ = dot2(f[i], ONES, csQ);
      }
    });
  }
  bar<L::barN(12)>();
  TS(12);
  if constexpr (CH == 0) {
    if constexpr (ROLE == 0) pageW2(6, 4);
  } else {
    if (g < 2) {
      f32x16 kv = {0};
      const int T0 = 4 * hh + q4;
      const unsigned gs0 = (T0 & 5) | ((T0 & 2) << 2);
      const unsigned a0 = L_PHI + T0 * 128 + (((8 * g + 4 * blk + p4) ^ gs0) << 3);
      const unsigned a1 = L_PHI + (T0 + 8) * 128 + (((8 * g + 4 * blk + p4) ^ gs0 ^ 2) << 3);
#pragma unroll
      for (int s = 0; s < 8; s++) kv = mfma(ldtr(lds + a0 + s * 2048, lds + a1 + s * 2048), ld128(lds + L_VFR + s * 1024 + l16), kv);
      const unsigned so = WS_KVP + ((c * 4 + 2 * g) * 16 + r) * 1024;
      vstore<0>(pack8(kv, 0), l16, sWS, so);
      vstore<0>(pack8(kv, 8), l16, sWS, so + 16384);
    }
    sfor<16, 28>([&](auto S) {
      constexpr int s = S, kc = s >> 2, tt = s & 3;
      const u32x4 f = ld128(lds + rot[tt] * 1024 + l16 + (5 * SLOT + (kc - 4) * 4096));
      accQ = mfma(f, xf[4 * kc + tt], accQ);
      if constexpr (tt == 0) {
#pragma unroll
        for (int i = 0; i < 4; i++) csQ = dot2(f[i], ONES, csQ);
      }
    });
    *(LDS3 float*)(lds + L_CS + 2048 + l16 + g * 4) = csQ;
    if constexpr (ROLE == 0) { if (w == 7) poll(ws, WS_AF + (R >> 1) * 128 + (l < 14 ? l : 0) * 8, tag); }
  }
  bar<L::barN(13)>();
  TS(13);
  u32x4 al[14][2];
  const unsigned actr = (2 * R + CH) * 144 * 1024 + 2 * g * 1024 + l16;
  if constexpr (ROLE == 0) {
    if constexpr (CH == 0) pageW2(7, 5);
    sfor<0, LA>([&](auto U) { constexpr int u = U; vload<true, 0>(al[u][0], actr, sWS, WS_ACT + u * 8192); vload<true, 1024>(al[u][1], actr, sWS, WS_ACT + u * 8192); });
  }
  if constexpr (CH == 1) {
    if (w == 7) flag(ws, WS_KF + c * 128 + r * 8, tag, l);
    const float t = sum4(*(LDS3 f32x4*)(lds + L_CS + 2048 + l16));
    accQ = mfma(cfrag(t, 0u), mfrag, accQ);
    f32x16 p, n;
    dual_softmax(accQ, rl, 1.f, hh != 0, p, n);
    phq[0] = pack8(p, 0); phq[1] = pack8(p, 8); phq[2] = pack8(n, 0); phq[3] = pack8(n, 8);
  }

  u32x4 t4[8];
  auto t4_load = [&]() {
    const unsigned so = WS_KVP + ((c * 4 + g) * 16 + 8 * CH) * 1024;
    vload<true, 0>(t4[0], l16, sWS, so); vload<true, 1024>(t4[1], l16, sWS, so); vload<true, 2048>(t4[2], l16, sWS, so); vload<true, 3072>(t4[3], l16, sWS, so);
    vload<true, 0>(t4[4], l16, sWS, so + 4096); vload<true, 1024>(t4[5], l16, sWS, so + 4096); vload<true, 2048>(t4[6], l16, sWS, so + 4096); vload<true, 3072>(t4[7], l16, sWS, so + 4096);
  };
  auto t4_sum = [&]() {
    f32x4 s0 = {0, 0, 0, 0}, s1 = {0, 0, 0, 0};
    unsigned KL = 0x00003F80u, KH = 0x3F800000u;
    asm volatile("" : "+s"(KL), "+s"(KH));
#pragma unroll
    for (int i = 0; i < 8; i++) {
      s0[0] = dot2(t4[i][0], KL, s0[0]); s0[1] = dot2(t4[i][0], KH, s0[1]);
      s0[2] = dot2(t4[i][1], KL, s0[2]); s0[3] = dot2(t4[i][1], KH, s0[3]);
      s1[0] = dot2(t4[i][2], KL, s1[0]); s1[1] = dot2(t4[i][2], KH, s1[1]);
      s1[2] = dot2(t4[i][3], KL, s1[2]); s1[3] = dot2(t4[i][3], KH, s1[3]);
    }
    *(LDS3 f32x4*)(lds + L_PHI + w * 2048 + l16) = s0;
    *(LDS3 f32x4*)(lds + L_PHI + w * 2048 + 1024 + l16) = s1;
  };
  auto t4_o = [&]() {
    f32x16 o = {0};
#pragma unroll
    for (int f = 0; f < 4; f++) {
      const f32x4 a0 = *(LDS3 f32x4*)(lds + L_PHI + f * 2048 + l16) + *(LDS3 f32x4*)(lds + L_PHI + (f + 4) * 2048 + l16);
      const f32x4 a1 = *(LDS3 f32x4*)(lds + L_PHI + f * 2048 + 1024 + l16) + *(LDS3 f32x4*)(lds + L_PHI + (f + 4) * 2048 + 1024 + l16);
      o = mfma(u32x4{pk2(a0[0], a0[1]), pk2(a0[2], a0[3]), pk2(a1[0], a1[1]), pk2(a1[2], a1[3])}, phq[f], o);
    }
    auto sw = __builtin_amdgcn_permlane32_swap(__float_as_uint(o[12]), __float_as_uint(o[12]), false, false);
    const float rc = __builtin_amdgcn_rcpf(__uint_as_float(sw[1]) + 1e-6f);
#pragma unroll
    for (int v = 0; v < 16; v++) o[v] = (v >= 12 && hh) ? 0.f : o[v] * rc;
    const unsigned so = WS_ACT + (112 + 2 * c) * 1024;
    vstore<0>(pack8(o, 0), actv, sWS, so);
    vstore<1024>(pack8(o, 8), actv, sWS, so);
  };

  if constexpr (ROLE == 1) {
    if constexpr (CH == 1) { if (w == 7) poll(ws, WS_KF + c * 128 + (l & 15) * 8, tag); }
    bar<63>();
    TS(14);
    t4_load();
    asm volatile("s_waitcnt vmcnt(0)" : "+v"(t4[0]), "+v"(t4[1]), "+v"(t4[2]), "+v"(t4[3]), "+v"(t4[4]), "+v"(t4[5]), "+v"(t4[6]), "+v"(t4[7]));
    t4_sum();
    bar<63>();
    TS(15);
    if constexpr (CH == 1) t4_o();
    bar<0>();
    TS(16);
    if constexpr (CH == 1) {
      if (w == 7) {
        flag(ws, WS_OF + r * 128 + c * 8, tag, l);
        if (r == 0 && c == 15) {
#pragma unroll
          for (int i = 0; i < 4; i++) poll(ws, WS_AF + i * 512 + ((l & 15) < 14 ? l : 0) * 8, tag);
          poll(ws, WS_KF + 14 * 128 + (l & 31) * 8, tag);
          flag(ws, 0, tag, l);
        }
      }
    }
  } else {
    f32x16 oa[2] = {{0}, {0}};
    unsigned ua[2];
#pragma unroll
    for (int i = 0; i < 2; i++) ua[i] = (4 * hh + q4) * 128 + ((((4 * i) ^ ((q4 >> 1) << 2)) + 2 * blk + (p4 >> 1)) << 4) + 8 * (p4 & 1);
    u32x4 of[8], xr;
    sfor<0, 14>([&](auto U) {
      constexpr int u = U, k = 14 + u;
      if constexpr (CH == 1 && k == KP) { if (w == 7) poll(ws, WS_KF + c * 128 + (l & 15) * 8, tag); }
      if constexpr (CH == 1 && k == OP) { if (w == 7) poll(ws, WS_OF + (R >> 1) * 128 + (l & 15) * 8, tag); }
      bar<L::barN(k)>();
      TS(k);
      if constexpr (CH == 1 && k == KP + 5) { if (w == 7) flag(ws, WS_OF + r * 128 + c * 8, tag, l); }
      constexpr unsigned su = ((u + 6) & 7) * SLOT;
      u32x4 a[2][2];
#pragma unroll
      for (int e = 0; e < 2; e++)
#pragma unroll
        for (int i = 0; i < 2; i++) a[e][i] = ldtr(lds + ua[i] + g * 4096 + (su + e * 2048), lds + ua[i] + g * 4096 + (su + e * 2048 + 1024));
      if constexpr (CH == 0) {
        if constexpr (u >= 1 && u <= 6) pageW2(7 + u, (u + 5) & 7);
        if constexpr (u >= 7 && u <= 10) pageWO(u - 7, u == 10 ? 2 : 4);
      }
      if constexpr (k == KP) t4_load();
      if constexpr (k == OP) {
        const unsigned so = WS_ACT + 112 * 1024;
        sfor<0, 4>([&](auto J) { constexpr int jj = J; vload<true, 0>(of[2 * jj], actr, sWS, so + jj * 8192); vload<true, 1024>(of[2 * jj + 1], actr, sWS, so + jj * 8192); });
        vload<false, 0>(xr, (64 * R + (threadIdx.x >> 3)) * 896 + (threadIdx.x & 7) * 16, sX, nt * 128);
      }
      if constexpr (u + LA <= 13) { vload<true, 0>(al[u + LA][0], actr, sWS, WS_ACT + (u + LA) * 8192); vload<true, 1024>(al[u + LA][1], actr, sWS, WS_ACT + (u + LA) * 8192); }
      asm volatile("s_waitcnt vmcnt(%2)" : "+v"(al[u][0]), "+v"(al[u][1]) : "n"(L::actN(u)));
#pragma unroll
      for (int e = 0; e < 2; e++)
#pragma unroll
        for (int i = 0; i < 2; i++) oa[i] = mfma(a[e][i], al[u][e], oa[i]);
      if constexpr (k == KP + 2) {
        asm volatile("s_waitcnt vmcnt(%8)" : "+v"(t4[0]), "+v"(t4[1]), "+v"(t4[2]), "+v"(t4[3]), "+v"(t4[4]), "+v"(t4[5]), "+v"(t4[6]), "+v"(t4[7]) : "n"(L::t4N()));
        t4_sum();
      }
      if constexpr (k == KP + 3 && CH == 1) t4_o();
    });
    bar<L::barN(28)>();
    TS(28);
    asm volatile("s_waitcnt vmcnt(%9)" : "+v"(of[0]), "+v"(of[1]), "+v"(of[2]), "+v"(of[3]), "+v"(of[4]), "+v"(of[5]), "+v"(of[6]), "+v"(of[7]), "+v"(xr) : "n"(L::oN()));
#pragma unroll
    for (int jj = 0; jj < 4; jj++)
#pragma unroll
      for (int t = 0; t < 2; t++)
#pragma unroll
        for (int i = 0; i < 2; i++) {
          LDS3 char* pb = lds + ua[i] + g * 3584 + (4 * SLOT + jj * 14336 + t * 2048);
          oa[i] = mfma(ldtr(pb, t == 0 ? pb + 1024 : pb + 1024 - hh * 512), of[2 * jj + t], oa[i]);
        }
    {
      const int T = 32 * CH + lr;
#pragma unroll
      for (int i = 0; i < 2; i++)
#pragma unroll
        for (int u = 0; u < 4; u++)
          *(LDS3 f32x4*)(lds + g * SLOT + T * 256 + (((8 * i + 2 * u + hh) ^ (T & 7)) << 4)) = f32x4{oa[i][4 * u], oa[i][4 * u + 1], oa[i][4 * u + 2], oa[i][4 * u + 3]};
    }
    bar<63>();
    TS(29);
    {
      const int T = threadIdx.x >> 3, ck = threadIdx.x & 7;
      f32x4 s0 = {0, 0, 0, 0}, s1 = {0, 0, 0, 0};
#pragma unroll
      for (int qq = 0; qq < 4; qq++) {
        s0 += *(LDS3 f32x4*)(lds + qq * SLOT + T * 256 + (((2 * ck) ^ (T & 7)) << 4));
        s1 += *(LDS3 f32x4*)(lds + qq * SLOT + T * 256 + (((2 * ck + 1) ^ (T & 7)) << 4));
      }
      u32x4 o;
      o[0] = pk2(s0[0] + bflo(xr[0]), s0[1] + bfhi(xr[0])); o[1] = pk2(s0[2] + bflo(xr[1]), s0[3] + bfhi(xr[1]));
      o[2] = pk2(s1[0] + bflo(xr[2]), s1[1] + bfhi(xr[2])); o[3] = pk2(s1[2] + bflo(xr[3]), s1[3] + bfhi(xr[3]));
      *(u32x4*)((char*)out + (64 * R + T) * 896 + nt * 128 + ck * 16) = o;
    }
  }
}

__global__ __launch_bounds__(512, 2) void fused_kernel(const u16* __restrict__ x, const u16* __restrict__ wqkv, const u16* __restrict__ wphi,
                                                       const u16* __restrict__ wo, const u16* __restrict__ w1, const u16* __restrict__ b1,
                                                       const u16* __restrict__ w2, u16* __restrict__ out, char* ws) {
  __shared__ __attribute__((aligned(16))) char lds_[LDS_BYTES];
  LDS3 char* lds = (LDS3 char*)lds_;
  const int w = __builtin_amdgcn_readfirstlane(threadIdx.x >> 6);
  const int b = blockIdx.x, xcd = b & 7, j = b >> 3, p = xcd >> 1;
  const int r = 4 * p + (j >> 3), c = 8 * (xcd & 1) + (j & 7);
  int R, nt;
  if ((xcd & 1) == 0) { R = 8 * p + (j >> 2); nt = j & 3; }
  else { const int m = (j >> 3) * 6 + (j & 7); R = 8 * p + m / 3; nt = 4 + m % 3; }
  if (c < 14) {
    if (w < 4) body<0, 0>(x, wqkv, wphi, wo, w1, b1, w2, out, ws, lds, w, r, c, R, nt);
    else body<0, 1>(x, wqkv, wphi, wo, w1, b1, w2, out, ws, lds, w, r, c, R, nt);
  } else {
    if (w < 4) body<1, 0>(x, wqkv, wphi, wo, w1, b1, w2, out, ws, lds, w, r, c, R, nt);
    else body<1, 1>(x, wqkv, wphi, wo, w1, b1, w2, out, ws, lds, w, r, c, R, nt);
  }
}

extern "C" void kernel_launch(void* const* d_in, const int* in_sizes, int n_in, void* d_out, int out_size, void* d_ws, size_t ws_size,
                              hipStream_t stream) {
  fused_kernel<<<256, 512, 0, stream>>>((const u16*)d_in[0], (const u16*)d_in[1], (const u16*)d_in[2], (const u16*)d_in[3], (const u16*)d_in[5],
                                        (const u16*)d_in[6], (const u16*)d_in[7], (u16*)d_out, (char*)d_ws);
}
```

```cpp
#include <hip/hip_runtime.h>
#include <stdint.h>
#include <type_traits>
#include <cstdio>
#define STAMP 1

typedef unsigned short u16;
typedef unsigned long long u64;
typedef __bf16 bf16x2 __attribute__((ext_vector_type(2)));
typedef __bf16 bf16x4 __attribute__((ext_vector_type(4)));
typedef __bf16 bf16x8 __attribute__((ext_vector_type(8)));
typedef float f32x2 __attribute__((ext_vector_type(2)));
typedef float f32x4 __attribute__((ext_vector_type(4)));
typedef float f32x16 __attribute__((ext_vector_type(16)));
typedef unsigned u32x2 __attribute__((ext_vector_type(2)));
typedef unsigned u32x4 __attribute__((ext_vector_type(4)));
#define LDS3 __attribute__((address_space(3)))
#define DEV __device__ __forceinline__

constexpr unsigned WS_PF = 1024, WS_AF = WS_PF + 2048, WS_KF = WS_AF + 2048, WS_OF = WS_KF + 2048;
constexpr unsigned WS_WP = 16384, WP_HEAD = 86016, WP_Q = 57344;
constexpr unsigned WS_KVP = WS_WP + 16 * WP_HEAD;
constexpr unsigned WS_ACT = WS_KVP + 16 * 4 * 16 * 1024;
constexpr unsigned WS_END = WS_ACT + 64 * 144 * 1024;
constexpr unsigned WS_DBG = WS_END;
constexpr unsigned SLOT = 16384;
constexpr unsigned L_PHI = 131072, L_VFR = L_PHI + 16384, L_ST = L_VFR + 8192, L_CS = L_ST + 2048;
constexpr unsigned LDS_BYTES = 163840;
#define LA 3
#define KP 16
#define OP 26
#define SPIN_MAX 2000000

template <int I, int N, class F> DEV void sfor(F&& f) {
  if constexpr (I < N) { f(std::integral_constant<int, I>{}); sfor<I + 1, N>(f); }
}
constexpr int cmin(int a, int b) { return a < b ? a : b; }
constexpr int cmax(int a, int b) { return a > b ? a : b; }

template <int ROLE, int CH> struct Led {
  static constexpr int pp = ROLE == 0 ? 8 : 4;
  static constexpr int nb1 = (ROLE == 0 && CH == 0) ? 2 : 0;
  static constexpr int pre = CH == 0 ? nb1 + 4 * pp : 0;
  static constexpr int G(int k) {
    if (CH != 0) return 0;
    if (k >= 1 && k <= 3) return pp;
    if (k == 5) return 8;
    if (k == 6) return 6;
    if (k == 7) return 7;
    if (ROLE != 0) return 0;
    if (k == 4) return 8;
    if (k >= 8 && k <= 13) return 4;
    if (k >= 15 && k <= 23) return 4;
    if (k == 24) return 2;
    return 0;
  }
  static constexpr int LX(int k) {
    if (ROLE != 0) return 0;
    return (k == KP ? 8 : 0) + (k == OP ? 9 : 0);
  }
  static constexpr int LAct(int k) {
    if (ROLE != 0) return 0;
    if (k == 13) return 2 * LA;
    if (k >= 14 && k - 14 + LA <= 13) return 2;
    return 0;
  }
  static constexpr int St(int k) {
    if (ROLE != 0) return 0;
    if (k == 7 || k == 8) return 2;
    if (CH == 1 && k == KP + 3) return 2;
    return 0;
  }
  static constexpr int cum(int k) { int s = pre; for (int i = 0; i < k; i++) s += G(i) + LX(i) + LAct(i) + St(i); return s; }
  static constexpr int need(int k) {
    if (CH == 0) {
      if (k <= 3) return nb1 + (k + 1) * pp;
      if (k <= 6) return cum(k - 3) + pp;
      if (k == 7) return cum(5) + 4;
      if (k == 8) return cum(5) + 8;
      if (k == 9) return cum(6) + 4;
      if (k == 10) return cum(6) + 6;
      if (k == 11) return ROLE == 0 ? cum(9) : cum(7) + 4;
      if (k == 12) return ROLE == 0 ? cum(9) : cum(7) + 7;
      if (ROLE != 0) return -1;
      if (k >= 16 && k <= 21) return cum(k - 8) + 4;
      if (k >= 22 && k <= 27) return cum(k - 7) + 4;
      if (k == 28) return cum(24) + 2;
      return -1;
    } else {
      if (k == 2 || k == 11 || k == 13) return cum(k);
      if (ROLE == 0 && k == KP + 5) return cum(KP + 4);
      return -1;
    }
  }
  static constexpr int barN(int k) { int n = need(k); return n < 0 ? 63 : cmin(63, cum(k) - n); }
  static constexpr int mid(int k) { return cum(k) + G(k) + LX(k) + LAct(k); }
  static constexpr int actEnd(int u) { return u < LA ? cum(13) + G(13) + LX(13) + 2 * (u + 1) : mid(14 + u - LA); }
  static constexpr int actN(int u) { return cmin(63, mid(14 + u) - actEnd(u)); }
  static constexpr int t4N() { return cmin(63, mid(KP + 2) - (cum(KP) + G(KP) + 8)); }
  static constexpr int oN() { return cmin(63, cum(28) - (cum(OP) + G(OP) + LX(OP))); }
};

DEV u32x4 mksrd(const void* p, unsigned bytes) {
  u64 a = (u64)p;
  u32x4 s; s[0] = (unsigned)a; s[1] = (unsigned)(a >> 32) & 0xffffu; s[2] = bytes; s[3] = 0x00020000u;
  return s;
}
template <bool SC1> DEV void glds(unsigned m0, unsigned voff, u32x4 srd, unsigned soff) {
  if constexpr (SC1)
    asm volatile("s_mov_b32 m0, %0\n\ts_nop 0\n\tbuffer_load_dwordx4 %1, %2, %3 offen sc1 lds" ::"s"(m0), "v"(voff), "s"(srd), "s"(soff) : "memory");
  else
    asm volatile("s_mov_b32 m0, %0\n\ts_nop 0\n\tbuffer_load_dwordx4 %1, %2, %3 offen lds" ::"s"(m0), "v"(voff), "s"(srd), "s"(soff) : "memory");
}
template <bool SC1, int OFF> DEV void vload(u32x4& d, unsigned voff, u32x4 srd, unsigned soff) {
  if constexpr (SC1)
    asm volatile("buffer_load_dwordx4 %0, %1, %2, %3 offen offset:%4 sc1" : "=v"(d) : "v"(voff), "s"(srd), "s"(soff), "n"(OFF) : "memory");
  else
    asm volatile("buffer_load_dwordx4 %0, %1, %2, %3 offen offset:%4" : "=v"(d) : "v"(voff), "s"(srd), "s"(soff), "n"(OFF) : "memory");
}
template <int OFF> DEV void vstore(u32x4 d, unsigned voff, u32x4 srd, unsigned soff) {
  asm volatile("buffer_store_dwordx4 %0, %1, %2, %3 offen offset:%4 sc1\n\ts_nop 1" ::"v"(d), "v"(voff), "s"(srd), "s"(soff), "n"(OFF) : "memory");
}
template <int N> DEV void bar() { asm volatile("s_waitcnt vmcnt(%0) lgkmcnt(0)\n\ts_barrier" ::"n"(N) : "memory"); }

DEV f32x16 mfma(u32x4 a, u32x4 b, f32x16 c) {
  return __builtin_amdgcn_mfma_f32_32x32x16_bf16(__builtin_bit_cast(bf16x8, a), __builtin_bit_cast(bf16x8, b), c, 0, 0, 0);
}
DEV float dot2(unsigned a, unsigned b, float c) {
  return __builtin_amdgcn_fdot2_f32_bf16(__builtin_bit_cast(bf16x2, a), __builtin_bit_cast(bf16x2, b), c, false);
}
DEV unsigned pk2(float lo, float hi) { f32x2 v = {lo, hi}; return __builtin_bit_cast(unsigned, __builtin_convertvector(v, bf16x2)); }
DEV float bflo(unsigned v) { return __uint_as_float(v << 16); }
DEV float bfhi(unsigned v) { return __uint_as_float(v & 0xffff0000u); }
DEV u32x4 ld128(LDS3 char* p) { return *(LDS3 u32x4*)p; }
DEV u32x4 ldtr(LDS3 char* p0, LDS3 char* p1) {
  bf16x4 a = __builtin_amdgcn_ds_read_tr16_b64_v4bf16((LDS3 bf16x4*)p0);
  bf16x4 b = __builtin_amdgcn_ds_read_tr16_b64_v4bf16((LDS3 bf16x4*)p1);
  return __builtin_bit_cast(u32x4, __builtin_shufflevector(a, b, 0, 1, 2, 3, 4, 5, 6, 7));
}
DEV u32x4 pack8(const f32x16& a, int o) {
  u32x4 r; r[0] = pk2(a[o], a[o + 1]); r[1] = pk2(a[o + 2], a[o + 3]); r[2] = pk2(a[o + 4], a[o + 5]); r[3] = pk2(a[o + 6], a[o + 7]);
  return r;
}
DEV float xhalf_max(float v) { auto s = __builtin_amdgcn_permlane32_swap(__float_as_uint(v), __float_as_uint(v), false, false); return fmaxf(__uint_as_float(s[0]), __uint_as_float(s[1])); }
DEV float xhalf_min(float v) { auto s = __builtin_amdgcn_permlane32_swap(__float_as_uint(v), __float_as_uint(v), false, false); return fminf(__uint_as_float(s[0]), __uint_as_float(s[1])); }
DEV float xhalf_add(float v) { auto s = __builtin_amdgcn_permlane32_swap(__float_as_uint(v), __float_as_uint(v), false, false); return __uint_as_float(s[0]) + __uint_as_float(s[1]); }
DEV u32x4 cfrag(float cs, unsigned e16) {
  unsigned hi = pk2(cs, cs);
  float lo = cs - bflo(hi);
  u32x4 r; r[0] = hi; r[1] = (pk2(lo, 0.f) & 0xffffu) | (e16 << 16); r[2] = e16; r[3] = 0;
  return r;
}
DEV float sum4(f32x4 v) { return (v[0] + v[1]) + (v[2] + v[3]); }

DEV void dual_softmax(const f32x16& a, float rl, float sc, bool hi_half, f32x16& p, f32x16& n) {
  float t[16];
#pragma unroll
  for (int v = 0; v < 16; v++) t[v] = (v >= 12 && hi_half) ? a[0] : a[v];
  float mx = t[0], mn = t[0];
#pragma unroll
  for (int v = 1; v < 16; v++) { mx = fmaxf(mx, t[v]); mn = fminf(mn, t[v]); }
  mx = xhalf_max(mx); mn = xhalf_min(mn);
  float c1 = -mx * rl, c2 = mn * rl, s1 = 0.f, s2 = 0.f;
#pragma unroll
  for (int v = 0; v < 16; v++) {
    float e1 = __builtin_amdgcn_exp2f(__builtin_fmaf(t[v], rl, c1));
    float e2 = __builtin_amdgcn_exp2f(__builtin_fmaf(-t[v], rl, c2));
    if (v >= 12) { e1 = hi_half ? 0.f : e1; e2 = hi_half ? 0.f : e2; }
    p[v] = e1; n[v] = e2; s1 += e1; s2 += e2;
  }
  s1 = sc * __builtin_amdgcn_rcpf(xhalf_add(s1));
  s2 = sc * __builtin_amdgcn_rcpf(xhalf_add(s2));
#pragma unroll
  for (int v = 0; v < 16; v++) { p[v] *= s1; n[v] *= s2; }
}

DEV void poll(const char* ws, unsigned off, u64 tag) {
  const u64* p = (const u64*)(ws + off);
#pragma unroll 1
  for (int it = 0; it < SPIN_MAX; it++) {
    const u64 v = __hip_atomic_load(p, __ATOMIC_RELAXED, __HIP_MEMORY_SCOPE_AGENT);
    if (__builtin_amdgcn_ballot_w64(v != tag) == 0) break;
    __builtin_amdgcn_s_sleep(1);
  }
}
DEV void flag(char* ws, unsigned off, u64 tag, int l) {
  if (l == 0) __hip_atomic_store((u64*)(ws + off), tag, __ATOMIC_RELAXED, __HIP_MEMORY_SCOPE_AGENT);
}

#define TS(k) do { unsigned long long t_ = __builtin_amdgcn_s_memtime(); asm volatile("v_writelane_b32 %0, %1, %2" : "+v"(stampv) : "s"((unsigned)t_), "n"(k)); } while (0)
#define TR(k) do { unsigned long long t_ = __builtin_amdgcn_s_memrealtime(); asm volatile("v_writelane_b32 %0, %1, %2" : "+v"(stampv) : "s"((unsigned)t_), "n"(k)); } while (0)

template <int ROLE, int CH>
DEV void body(const u16* __restrict__ x, const u16* __restrict__ wqkv, const u16* __restrict__ wphi, const u16* __restrict__ wo,
              const u16* __restrict__ w1, const u16* __restrict__ b1, const u16* __restrict__ w2, u16* __restrict__ out, char* ws,
              LDS3 char* lds, const int w, const int r, const int c, const int R, const int nt) {
  using L = Led<ROLE, CH>;
  const int l = threadIdx.x & 63, lr = l & 31, hh = l >> 5;
  const int g = w & 3;
  const int q4 = (l & 15) >> 2, p4 = l & 3, blk = (l >> 4) & 1;
  const unsigned ldsb = (unsigned)(size_t)lds;
  unsigned stampv = 0;
  TS(40); TR(42);
  const u32x4 sX = mksrd(x, 2048 * 896), sW1 = mksrd(w1, 448 * 3584), sW2 = mksrd(w2, 1792 * 896), sWO = mksrd(wo, 448 * 896);
  const u32x4 sWS = mksrd(ws, WS_END);
  const unsigned m0w = ldsb + g * 1024;
  const unsigned vX = (8 * g + (l >> 3)) * 896 + (((l & 7) ^ (((g & 1) << 2) | (l >> 4))) << 4);
  const unsigned vW1 = (4 * g + (l >> 4)) * 3584 + (((l & 15) ^ ((l >> 4) << 2)) << 4);
  const unsigned vW2 = (8 * g + (l >> 3)) * 896 + (((l & 7) ^ (((l >> 4) & 1) << 2)) << 4);
  const unsigned vLin = g * 1024 + l * 16;
  const unsigned l16 = l * 16;

  auto pageX = [&](int kc, int slot) {
#pragma unroll
    for (int j = 0; j < 4; j++) glds<false>(m0w + slot * SLOT + j * 4096, vX, sX, (128 * r + 32 * j) * 896 + kc * 128);
  };
  auto pageW1 = [&](int kc, int slot) {
#pragma unroll
    for (int j = 0; j < 4; j++) glds<false>(m0w + slot * SLOT + j * 4096, vW1, sW1, (64 * kc + 16 * j) * 3584 + c * 256);
  };
  auto pageW2 = [&](int u, int slot) {
#pragma unroll
    for (int j = 0; j < 4; j++) glds<false>(m0w + slot * SLOT + j * 4096, vW2, sW2, (128 * u + 32 * j) * 896 + nt * 128);
  };
  auto pageWO = [&](int u, int n) {
#pragma unroll
    for (int j = 0; j < n; j++) glds<false>(m0w + (4 + u) * SLOT + j * 4096, vW2, sWO, (128 * u + 32 * j) * 896 + nt * 128);
  };
  auto pageLin = [&](unsigned off, int slot, int n) {
#pragma unroll
    for (int j = 0; j < n; j++) glds<true>(m0w + slot * SLOT + j * 4096, vLin, sWS, WS_WP + c * WP_HEAD + off + j * 4096);
  };

  unsigned b1v[2] = {0, 0};
  u64 tag = 0;
  u32x4 pA0 = {0, 0, 0, 0}, pA1 = {0, 0, 0, 0}, pB0 = {0, 0, 0, 0}, pB1 = {0, 0, 0, 0};
  if constexpr (CH == 0) {
    if constexpr (ROLE == 0) {
      const u32x4 sB1 = mksrd(b1, 1792 * 2);
      const unsigned vb = (128 * c + lr) * 2;
      asm volatile("buffer_load_ushort %0, %1, %2, 0 offen" : "=v"(b1v[0]) : "v"(vb), "s"(sB1) : "memory");
      asm volatile("buffer_load_ushort %0, %1, %2, 0 offen offset:64" : "=v"(b1v[1]) : "v"(vb), "s"(sB1) : "memory");
    }
#pragma unroll
    for (int kc = 0; kc < 4; kc++) { pageX(kc, 2 * kc); if constexpr (ROLE == 0) pageW1(kc, 2 * kc + 1); }
  } else {
    if (w == 7) tag = __hip_atomic_load((const u64*)ws, __ATOMIC_RELAXED, __HIP_MEMORY_SCOPE_AGENT) + 1;
    if constexpr (ROLE == 0) {
      b1v[0] = b1[128 * c + 64 + lr]; b1v[1] = b1[128 * c + 96 + lr];
      if (w != 7) {
        const int kk = (lr & 0x13) | ((lr & 4) << 1) | ((lr & 8) >> 1);
        const char* a = (const char*)wqkv + (32 * c + kk) * 2688 + (g * 448 + 28 * r + 8 * hh) * 2;
        u32x2 a0 = *(const u32x2*)a, a1 = *(const u32x2*)(a + 8), a2 = *(const u32x2*)(a + 32), a3 = *(const u32x2*)(a + (hh ? 32 : 40));
        pA0 = u32x4{a0[0], a0[1], a1[0], a1[1]}; pA1 = u32x4{a2[0], a2[1], a3[0], a3[1]};
        if (g < 2) {
          __amdgpu_buffer_rsrc_t rp = __builtin_amdgcn_make_buffer_rsrc((void*)wphi, 0, 16 * 784 * 2, 0x00020000);
          unsigned e[16];
#pragma unroll
          for (int i = 0; i < 16; i++) {
            const int d = (i >> 3) * 16 + 8 * hh + (i & 7);
            const unsigned o = (d < 28 && lr < 28) ? (unsigned)(r * 784 + d * 28 + lr) * 2u : 0x40000000u;
            e[i] = __builtin_amdgcn_raw_buffer_load_b16(rp, o, 0, 0);
          }
#pragma unroll
          for (int i = 0; i < 4; i++) { pB0[i] = e[2 * i] | (e[2 * i + 1] << 16); pB1[i] = e[8 + 2 * i] | (e[9 + 2 * i] << 16); }
        } else {
#pragma unroll
          for (int i = 0; i < 4; i++) {
            const int d = 8 * hh + 2 * i;
            pB0[i] = (lr == d ? 0x3F80u : 0u) | (lr == d + 1 ? 0x3F800000u : 0u);
            pB1[i] = lr < 28 ? ((lr == d + 16 ? 0x3F80u : 0u) | (lr == d + 17 ? 0x3F800000u : 0u)) : 0u;
          }
        }
      }
    }
  }

  unsigned xa[4], wa[4][2], rot[4];
#pragma unroll
  for (int tt = 0; tt < 4; tt++) {
    const int s = (tt + g) & 3, row = 32 * g + lr;
    xa[tt] = row * 128 + (((2 * s + hh) ^ ((row >> 1) & 7)) << 4);
#pragma unroll
    for (int i = 0; i < 2; i++) wa[tt][i] = (16 * s + 8 * hh + q4) * 256 + ((((8 * CH + 4 * i) ^ (q4 << 2)) + 2 * blk + (p4 >> 1)) << 4) + 8 * (p4 & 1);
    rot[tt] = s;
  }

  u32x4 xf[28];
  f32x16 acc[2] = {{0}, {0}};
  float cs[2] = {0.f, 0.f}, st0 = 0.f, st1 = 0.f;
  const unsigned ONES = 0x3F803F80u;

  sfor<0, 7>([&](auto KC) {
    constexpr int kc = KC;
    if constexpr (CH == 1 && kc == 5) { if (w == 7) poll(ws, WS_PF + c * 128 + (l < 14 ? l : 0) * 8, tag); }
    bar<L::barN(kc)>();
    TS(kc);
    if constexpr (CH == 0 && ROLE == 0 && kc == 0) asm volatile("" : "+v"(b1v[0]), "+v"(b1v[1]));
    if constexpr (CH == 1 && ROLE == 0 && kc == 2) { if (w == 7) flag(ws, WS_PF + r * 128 + c * 8, tag, l); }
    constexpr unsigned sx = ((2 * kc) & 7) * SLOT, sw = sx + SLOT;
    u32x4 af[4][2];
    sfor<0, 4>([&](auto TT) {
      constexpr int tt = TT;
      xf[4 * kc + tt] = ld128(lds + xa[tt] + sx);
      if constexpr (ROLE == 0) {
        af[tt][0] = ldtr(lds + wa[tt][0] + sw, lds + wa[tt][0] + sw + 1024);
        af[tt][1] = ldtr(lds + wa[tt][1] + sw, lds + wa[tt][1] + sw + 1024);
      }
      if constexpr (tt == 0) {
        if constexpr (CH == 0) {
          if constexpr (kc >= 1 && kc <= 3) { pageX(kc + 3, (2 * kc + 6) & 7); if constexpr (ROLE == 0) pageW1(kc + 3, (2 * kc + 7) & 7); }
          if constexpr (kc == 4 && ROLE == 0) { pageW2(0, 6); pageW2(1, 7); }
          if constexpr (kc == 5) { pageLin(0, 0, 4); pageLin(16384, 1, 4); }
          if constexpr (kc == 6) { pageLin(32768, 2, 4); pageLin(49152, 3, 2); }
        } else if constexpr (ROLE == 0 && kc == 0) {
          asm volatile("" : "+v"(b1v[0]), "+v"(b1v[1]));
          if (w != 7) {
            f32x16 d = {0};
            d = mfma(pA0, pB0, d); d = mfma(pA1, pB1, d);
            const unsigned so = WS_WP + r * WP_HEAD + (g == 0 ? WP_Q + 2 * c * 1024 : 2 * c * 2048 + (g - 1) * 1024);
            vstore<0>(pack8(d, 0), l16, sWS, so);
            vstore<0>(pack8(d, 8), l16, sWS, so + (g == 0 ? 1024 : 2048));
          }
        }
      }
      const u32x4 xv = xf[4 * kc + tt];
      if constexpr (ROLE == 0) {
        acc[0] = mfma(af[tt][0], xv, acc[0]);
        acc[1] = mfma(af[tt][1], xv, acc[1]);
        if constexpr (tt == 0) {
#pragma unroll
          for (int i = 0; i < 4; i++) { cs[0] = dot2(af[0][0][i], ONES, cs[0]); cs[1] = dot2(af[0][1][i], ONES, cs[1]); }
        }
      }
      st0 = dot2(xv[0], CH == 0 ? ONES : xv[0], st0); st1 = dot2(xv[1], CH == 0 ? ONES : xv[1], st1);
      st0 = dot2(xv[2], CH == 0 ? ONES : xv[2], st0); st1 = dot2(xv[3], CH == 0 ? ONES : xv[3], st1);
    });
  });
  *(LDS3 float*)(lds + L_ST + CH * 1024 + (32 * g + lr) * 8 + hh * 4) = st0 + st1;
  if constexpr (ROLE == 0) {
    *(LDS3 float*)(lds + L_CS + (2 * CH) * 1024 + l16 + g * 4) = cs[0];
    *(LDS3 float*)(lds + L_CS + (2 * CH + 1) * 1024 + l16 + g * 4) = cs[1];
  }

  u32x4 mfrag;
  float rr, rl;
  f32x16 accB = {0};
  float csB = 0.f;
  const unsigned actv = (4 * r + g) * 144 * 1024 + l16;
  auto mish8 = [&](const f32x16& a, int o) {
    float t[8];
#pragma unroll
    for (int j = 0; j < 8; j++) {
      const float u = __builtin_amdgcn_exp2f(a[o + j] * rl);
      const float d = __builtin_fmaf(u, u + 2.f, 2.f);
      const float k = __builtin_fmaf(-2.f, __builtin_amdgcn_rcpf(d), 1.f);
      t[j] = (a[o + j] * rr) * k;
    }
    return u32x4{pk2(t[0], t[1]), pk2(t[2], t[3]), pk2(t[4], t[5]), pk2(t[6], t[7])};
  };
  sfor<0, 4>([&](auto V) {
    constexpr int v = V, k = 7 + v;
    bar<L::barN(k)>();
    TS(k);
    if constexpr (v == 0) {
      const f32x2 s1 = *(LDS3 f32x2*)(lds + L_ST + (32 * g + lr) * 8), s2 = *(LDS3 f32x2*)(lds + L_ST + 1024 + (32 * g + lr) * 8);
      const float m = (s1[0] + s1[1]) * (1.f / 448.f);
      const float var = fmaxf((s2[0] + s2[1]) * (1.f / 448.f) - m * m, 0.f) + 1e-5f;
      rr = __builtin_amdgcn_rsqf(var);
      const float sg = var * rr;
      rl = rr * 1.44269504088896f;
      const unsigned mh = pk2(-m, sg);
      const unsigned ml = pk2(-m - bflo(mh), sg - bfhi(mh));
      mfrag[0] = (mh & 0xffffu) | (ml << 16); mfrag[1] = mh; mfrag[2] = ml >> 16; mfrag[3] = 0;
      if constexpr (ROLE == 0) {
#pragma unroll
        for (int i = 0; i < 2; i++) {
          const float t = sum4(*(LDS3 f32x4*)(lds + L_CS + (2 * CH + i) * 1024 + l16));
          acc[i] = mfma(cfrag(t, hh ? 0u : b1v[i]), mfrag, acc[i]);
        }
      }
    }
    sfor<0, (v == 3 ? 4 : 8)>([&](auto S) {
      constexpr int s = S, kc = 2 * v + (s >> 2), tt = s & 3;
      const u32x4 f = ld128(lds + rot[tt] * 2048 + l16 + (CH * 1024 + v * SLOT + (kc & 1) * 8192));
      if constexpr (s == 0 && CH == 0) {
        if constexpr (v == 0) { pageLin(WP_Q, 4, 4); pageLin(WP_Q + 16384, 5, 3); }
        else if constexpr (ROLE == 0) pageW2(1 + v, v - 1);
      }
      if constexpr (CH == 0) accB = mfma(f, xf[4 * kc + tt], accB); else accB = mfma(xf[4 * kc + tt], f, accB);
      if constexpr (tt == 0) {
#pragma unroll
        for (int i = 0; i < 4; i++) csB = dot2(f[i], ONES, csB);
      }
    });
    if constexpr (ROLE == 0 && v < 2) {
      const unsigned so = WS_ACT + (8 * c + 4 * CH + 2 * v) * 1024;
      vstore<0>(mish8(acc[v], 0), actv, sWS, so);
      vstore<1024>(mish8(acc[v], 8), actv, sWS, so);
    }
  });
  *(LDS3 float*)(lds + L_CS + CH * 1024 + l16 + g * 4) = csB;

  bar<L::barN(11)>();
  TS(11);
  f32x16 accQ = {0};
  float csQ = 0.f;
  u32x4 phq[4];
  if constexpr (CH == 0) {
    const float t = sum4(*(LDS3 f32x4*)(lds + L_CS + l16));
    if constexpr (ROLE == 0) pageW2(5, 3);
    accB = mfma(cfrag(t, 0u), mfrag, accB);
    f32x16 p, n;
    dual_softmax(accB, rl, rr, hh != 0, p, n);
    const int T = 32 * g + lr;
    const unsigned gs = (T & 5) | ((T & 2) << 2) | ((T & 8) >> 2);
#pragma unroll
    for (int u = 0; u < 4; u++) {
      *(LDS3 u32x2*)(lds + L_PHI + T * 128 + (((2 * u + hh) ^ gs) << 3)) = u32x2{pk2(p[4 * u], p[4 * u + 1]), pk2(p[4 * u + 2], p[4 * u + 3])};
      *(LDS3 u32x2*)(lds + L_PHI + T * 128 + (((8 + 2 * u + hh) ^ gs) << 3)) = u32x2{pk2(n[4 * u], n[4 * u + 1]), pk2(n[4 * u + 2], n[4 * u + 3])};
    }
  } else {
    if constexpr (ROLE == 0) { if (w == 7) flag(ws, WS_AF + r * 128 + c * 8, tag, l); }
    const float t = sum4(*(LDS3 f32x4*)(lds + L_CS + 1024 + l16));
    accB = mfma(mfrag, cfrag(t, l == 28 ? 0x3F80u : 0u), accB);
    *(LDS3 u32x4*)(lds + L_VFR + 2 * g * 1024 + l16) = pack8(accB, 0);
    *(LDS3 u32x4*)(lds + L_VFR + (2 * g + 1) * 1024 + l16) = pack8(accB, 8);
    sfor<0, 16>([&](auto S) {
      constexpr int s = S, kc = s >> 2, tt = s & 3;
      const u32x4 f = ld128(lds + rot[tt] * 1024 + l16 + (4 * SLOT + kc * 4096));
      accQ = mfma(f, xf[4 * kc + tt], accQ);
      if constexpr (tt == 0) {
#pragma unroll
        for (int i = 0; i < 4; i++) csQ = dot2(f[i], ONES, csQ);
      }
    });
  }
  bar<L::barN(12)>();
  TS(12);
  if constexpr (CH == 0) {
    if constexpr (ROLE == 0) pageW2(6, 4);
  } else {
    if (g < 2) {
      f32x16 kv = {0};
      const int T0 = 4 * hh + q4;
      const unsigned gs0 = (T0 & 5) | ((T0 & 2) << 2);
      const unsigned a0 = L_PHI + T0 * 128 + (((8 * g + 4 * blk + p4) ^ gs0) << 3);
      const unsigned a1 = L_PHI + (T0 + 8) * 128 + (((8 * g + 4 * blk + p4) ^ gs0 ^ 2) << 3);
#pragma unroll
      for (int s = 0; s < 8; s++) kv = mfma(ldtr(lds + a0 + s * 2048, lds + a1 + s * 2048), ld128(lds + L_VFR + s * 1024 + l16), kv);
      const unsigned so = WS_KVP + ((c * 4 + 2 * g) * 16 + r) * 1024;
      vstore<0>(pack8(kv, 0), l16, sWS, so);
      vstore<0>(pack8(kv, 8), l16, sWS, so + 16384);
    }
    sfor<16, 28>([&](auto S) {
      constexpr int s = S, kc = s >> 2, tt = s & 3;
      const u32x4 f = ld128(lds + rot[tt] * 1024 + l16 + (5 * SLOT + (kc - 4) * 4096));
      accQ = mfma(f, xf[4 * kc + tt], accQ);
      if constexpr (tt == 0) {
#pragma unroll
        for (int i = 0; i < 4; i++) csQ = dot2(f[i], ONES, csQ);
      }
    });
    *(LDS3 float*)(lds + L_CS + 2048 + l16 + g * 4) = csQ;
    if constexpr (ROLE == 0) { if (w == 7) poll(ws, WS_AF + (R >> 1) * 128 + (l < 14 ? l : 0) * 8, tag); }
  }
  bar<L::barN(13)>();
  TS(13);
  u32x4 al[14][2];
  const unsigned actr = (2 * R + CH) * 144 * 1024 + 2 * g * 1024 + l16;
  if constexpr (ROLE == 0) {
    if constexpr (CH == 0) pageW2(7, 5);
    sfor<0, LA>([&](auto U) { constexpr int u = U; vload<true, 0>(al[u][0], actr, sWS, WS_ACT + u * 8192); vload<true, 1024>(al[u][1], actr, sWS, WS_ACT + u * 8192); });
  }
  if constexpr (CH == 1) {
    if (w == 7) flag(ws, WS_KF + c * 128 + r * 8, tag, l);
    const float t = sum4(*(LDS3 f32x4*)(lds + L_CS + 2048 + l16));
    accQ = mfma(cfrag(t, 0u), mfrag, accQ);
    f32x16 p, n;
    dual_softmax(accQ, rl, 1.f, hh != 0, p, n);
    phq[0] = pack8(p, 0); phq[1] = pack8(p, 8); phq[2] = pack8(n, 0); phq[3] = pack8(n, 8);
  }

  u32x4 t4[8];
  auto t4_load = [&]() {
    const unsigned so = WS_KVP + ((c * 4 + g) * 16 + 8 * CH) * 1024;
    vload<true, 0>(t4[0], l16, sWS, so); vload<true, 1024>(t4[1], l16, sWS, so); vload<true, 2048>(t4[2], l16, sWS, so); vload<true, 3072>(t4[3], l16, sWS, so);
    vload<true, 0>(t4[4], l16, sWS, so + 4096); vload<true, 1024>(t4[5], l16, sWS, so + 4096); vload<true, 2048>(t4[6], l16, sWS, so + 4096); vload<true, 3072>(t4[7], l16, sWS, so + 4096);
  };
  auto t4_sum = [&]() {
    f32x4 s0 = {0, 0, 0, 0}, s1 = {0, 0, 0, 0};
    unsigned KL = 0x00003F80u, KH = 0x3F800000u;
    asm volatile("" : "+s"(KL), "+s"(KH));
#pragma unroll
    for (int i = 0; i < 8; i++) {
      s0[0] = dot2(t4[i][0], KL, s0[0]); s0[1] = dot2(t4[i][0], KH, s0[1]);
      s0[2] = dot2(t4[i][1], KL, s0[2]); s0[3] = dot2(t4[i][1], KH, s0[3]);
      s1[0] = dot2(t4[i][2], KL, s1[0]); s1[1] = dot2(t4[i][2], KH, s1[1]);
      s1[2] = dot2(t4[i][3], KL, s1[2]); s1[3] = dot2(t4[i][3], KH, s1[3]);
    }
    *(LDS3 f32x4*)(lds + L_PHI + w * 2048 + l16) = s0;
    *(LDS3 f32x4*)(lds + L_PHI + w * 2048 + 1024 + l16) = s1;
  };
  auto t4_o = [&]() {
    f32x16 o = {0};
#pragma unroll
    for (int f = 0; f < 4; f++) {
      const f32x4 a0 = *(LDS3 f32x4*)(lds + L_PHI + f * 2048 + l16) + *(LDS3 f32x4*)(lds + L_PHI + (f + 4) * 2048 + l16);
      const f32x4 a1 = *(LDS3 f32x4*)(lds + L_PHI + f * 2048 + 1024 + l16) + *(LDS3 f32x4*)(lds + L_PHI + (f + 4) * 2048 + 1024 + l16);
      o = mfma(u32x4{pk2(a0[0], a0[1]), pk2(a0[2], a0[3]), pk2(a1[0], a1[1]), pk2(a1[2], a1[3])}, phq[f], o);
    }
    auto sw = __builtin_amdgcn_permlane32_swap(__float_as_uint(o[12]), __float_as_uint(o[12]), false, false);
    const float rc = __builtin_amdgcn_rcpf(__uint_as_float(sw[1]) + 1e-6f);
#pragma unroll
    for (int v = 0; v < 16; v++) o[v] = (v >= 12 && hh) ? 0.f : o[v] * rc;
    const unsigned so = WS_ACT + (112 + 2 * c) * 1024;
    vstore<0>(pack8(o, 0), actv, sWS, so);
    vstore<1024>(pack8(o, 8), actv, sWS, so);
  };

  if constexpr (ROLE == 1) {
    if constexpr (CH == 1) { if (w == 7) poll(ws, WS_KF + c * 128 + (l & 15) * 8, tag); }
    bar<63>();
    TS(14);
    t4_load();
    asm volatile("s_waitcnt vmcnt(0)" : "+v"(t4[0]), "+v"(t4[1]), "+v"(t4[2]), "+v"(t4[3]), "+v"(t4[4]), "+v"(t4[5]), "+v"(t4[6]), "+v"(t4[7]));
    t4_sum();
    bar<63>();
    TS(15);
    if constexpr (CH == 1) t4_o();
    bar<0>();
    TS(16);
    if constexpr (CH == 1) {
      if (w == 7) {
        flag(ws, WS_OF + r * 128 + c * 8, tag, l);
        if (r == 0 && c == 15) {
#pragma unroll
          for (int i = 0; i < 4; i++) poll(ws, WS_AF + i * 512 + ((l & 15) < 14 ? l : 0) * 8, tag);
          poll(ws, WS_KF + 14 * 128 + (l & 31) * 8, tag);
          flag(ws, 0, tag, l);
        }
      }
    }
  } else {
    f32x16 oa[2] = {{0}, {0}};
    unsigned ua[2];
#pragma unroll
    for (int i = 0; i < 2; i++) ua[i] = (4 * hh + q4) * 128 + ((((4 * i) ^ ((q4 >> 1) << 2)) + 2 * blk + (p4 >> 1)) << 4) + 8 * (p4 & 1);
    u32x4 of[8], xr;
    sfor<0, 14>([&](auto U) {
      constexpr int u = U, k = 14 + u;
      if constexpr (CH == 1 && k == KP) { if (w == 7) poll(ws, WS_KF + c * 128 + (l & 15) * 8, tag); }
      if constexpr (CH == 1 && k == OP) { if (w == 7) poll(ws, WS_OF + (R >> 1) * 128 + (l & 15) * 8, tag); }
      bar<L::barN(k)>();
      TS(k);
      if constexpr (CH == 1 && k == KP + 5) { if (w == 7) flag(ws, WS_OF + r * 128 + c * 8, tag, l); }
      constexpr unsigned su = ((u + 6) & 7) * SLOT;
      u32x4 a[2][2];
#pragma unroll
      for (int e = 0; e < 2; e++)
#pragma unroll
        for (int i = 0; i < 2; i++) a[e][i] = ldtr(lds + ua[i] + g * 4096 + (su + e * 2048), lds + ua[i] + g * 4096 + (su + e * 2048 + 1024));
      if constexpr (CH == 0) {
        if constexpr (u >= 1 && u <= 6) pageW2(7 + u, (u + 5) & 7);
        if constexpr (u >= 7 && u <= 10) pageWO(u - 7, u == 10 ? 2 : 4);
      }
      if constexpr (k == KP) t4_load();
      if constexpr (k == OP) {
        const unsigned so = WS_ACT + 112 * 1024;
        sfor<0, 4>([&](auto J) { constexpr int jj = J; vload<true, 0>(of[2 * jj], actr, sWS, so + jj * 8192); vload<true, 1024>(of[2 * jj + 1], actr, sWS, so + jj * 8192); });
        vload<false, 0>(xr, (64 * R + (threadIdx.x >> 3)) * 896 + (threadIdx.x & 7) * 16, sX, nt * 128);
      }
      if constexpr (u + LA <= 13) { vload<true, 0>(al[u + LA][0], actr, sWS, WS_ACT + (u + LA) * 8192); vload<true, 1024>(al[u + LA][1], actr, sWS, WS_ACT + (u + LA) * 8192); }
      asm volatile("s_waitcnt vmcnt(%2)" : "+v"(al[u][0]), "+v"(al[u][1]) : "n"(L::actN(u)));
#pragma unroll
      for (int e = 0; e < 2; e++)
#pragma unroll
        for (int i = 0; i < 2; i++) oa[i] = mfma(a[e][i], al[u][e], oa[i]);
      if constexpr (k == KP + 2) {
        asm volatile("s_waitcnt vmcnt(%8)" : "+v"(t4[0]), "+v"(t4[1]), "+v"(t4[2]), "+v"(t4[3]), "+v"(t4[4]), "+v"(t4[5]), "+v"(t4[6]), "+v"(t4[7]) : "n"(L::t4N()));
        t4_sum();
      }
      if constexpr (k == KP + 3 && CH == 1) t4_o();
    });
    bar<L::barN(28)>();
    TS(28);
    asm volatile("s_waitcnt vmcnt(%9)" : "+v"(of[0]), "+v"(of[1]), "+v"(of[2]), "+v"(of[3]), "+v"(of[4]), "+v"(of[5]), "+v"(of[6]), "+v"(of[7]), "+v"(xr) : "n"(L::oN()));
#pragma unroll
    for (int jj = 0; jj < 4; jj++)
#pragma unroll
      for (int t = 0; t < 2; t++)
#pragma unroll
        for (int i = 0; i < 2; i++) {
          LDS3 char* pb = lds + ua[i] + g * 3584 + (4 * SLOT + jj * 14336 + t * 2048);
          oa[i] = mfma(ldtr(pb, t == 0 ? pb + 1024 : pb + 1024 - hh * 512), of[2 * jj + t], oa[i]);
        }
    {
      const int T = 32 * CH + lr;
#pragma unroll
      for (int i = 0; i < 2; i++)
#pragma unroll
        for (int u = 0; u < 4; u++)
          *(LDS3 f32x4*)(lds + g * SLOT + T * 256 + (((8 * i + 2 * u + hh) ^ (T & 7)) << 4)) = f32x4{oa[i][4 * u], oa[i][4 * u + 1], oa[i][4 * u + 2], oa[i][4 * u + 3]};
    }
    bar<63>();
    TS(29);
    {
      const int T = threadIdx.x >> 3, ck = threadIdx.x & 7;
      f32x4 s0 = {0, 0, 0, 0}, s1 = {0, 0, 0, 0};
#pragma unroll
      for (int qq = 0; qq < 4; qq++) {
        s0 += *(LDS3 f32x4*)(lds + qq * SLOT + T * 256 + (((2 * ck) ^ (T & 7)) << 4));
        s1 += *(LDS3 f32x4*)(lds + qq * SLOT + T * 256 + (((2 * ck + 1) ^ (T & 7)) << 4));
      }
      u32x4 o;
      o[0] = pk2(s0[0] + bflo(xr[0]), s0[1] + bfhi(xr[0])); o[1] = pk2(s0[2] + bflo(xr[1]), s0[3] + bfhi(xr[1]));
      o[2] = pk2(s1[0] + bflo(xr[2]), s1[1] + bfhi(xr[2])); o[3] = pk2(s1[2] + bflo(xr[3]), s1[3] + bfhi(xr[3]));
      *(u32x4*)((char*)out + (64 * R + T) * 896 + nt * 128 + ck * 16) = o;
    }
  }
  TS(41); TR(43);
  if (blockIdx.x == 16 && w == 0) {
    unsigned s40 = __builtin_amdgcn_readlane(stampv, 40);
    unsigned a0 = __builtin_amdgcn_readlane(stampv, 0) - s40, a1 = __builtin_amdgcn_readlane(stampv, 4) - s40, a2 = __builtin_amdgcn_readlane(stampv, 7) - s40, a3 = __builtin_amdgcn_readlane(stampv, 13) - s40,
     a4 = __builtin_amdgcn_readlane(stampv, 20) - s40, a5 = __builtin_amdgcn_readlane(stampv, 28) - s40, a6 = __builtin_amdgcn_readlane(stampv, 41) - s40, a7 = __builtin_amdgcn_readlane(stampv, 43) - __builtin_amdgcn_readlane(stampv, 42);
    if (l == 0 && (s40 & 0x70) == 0) printf("ST %u %u %u %u %u %u %u rt %u\n", a0, a1, a2, a3, a4, a5, a6, a7);
  }
}

__global__ __launch_bounds__(512, 2) void fused_kernel(const u16* __restrict__ x, const u16* __restrict__ wqkv, const u16* __restrict__ wphi,
                                                       const u16* __restrict__ wo, const u16* __restrict__ w1, const u16* __restrict__ b1,
                                                       const u16* __restrict__ w2, u16* __restrict__ out, char* ws) {
  __shared__ __attribute__((aligned(16))) char lds_[LDS_BYTES];
  LDS3 char* lds = (LDS3 char*)lds_;
  const int w = __builtin_amdgcn_readfirstlane(threadIdx.x >> 6);
  const int b = blockIdx.x, xcd = b & 7, j = b >> 3, p = xcd >> 1;
  const int r = 4 * p + (j >> 3), c = 8 * (xcd & 1) + (j & 7);
  int R, nt;
  if ((xcd & 1) == 0) { R = 8 * p + (j >> 2); nt = j & 3; }
  else { const int m = (j >> 3) * 6 + (j & 7); R = 8 * p + m / 3; nt = 4 + m % 3; }
  if (c < 14) {
    if (w < 4) body<0, 0>(x, wqkv, wphi, wo, w1, b1, w2, out, ws, lds, w, r, c, R, nt);
    else body<0, 1>(x, wqkv, wphi, wo, w1, b1, w2, out, ws, lds, w, r, c, R, nt);
  } else {
    if (w < 4) body<1, 0>(x, wqkv, wphi, wo, w1, b1, w2, out, ws, lds, w, r, c, R, nt);
    else body<1, 1>(x, wqkv, wphi, wo, w1, b1, w2, out, ws, lds, w, r, c, R, nt);
  }
}

extern "C" void kernel_launch(void* const* d_in, const int* in_sizes, int n_in, void* d_out, int out_size, void* d_ws, size_t ws_size,
                              hipStream_t stream) {
  fused_kernel<<<256, 512, 0, stream>>>((const u16*)d_in[0], (const u16*)d_in[1], (const u16*)d_in[2], (const u16*)d_in[3], (const u16*)d_in[5],
                                        (const u16*)d_in[6], (const u16*)d_in[7], (u16*)d_out, (char*)d_ws);
}
```

```cpp
#include <hip/hip_runtime.h>
#include <stdint.h>
#include <type_traits>
#define STAMP 1

typedef unsigned short u16;
typedef unsigned long long u64;
typedef __bf16 bf16x2 __attribute__((ext_vector_type(2)));
typedef __bf16 bf16x4 __attribute__((ext_vector_type(4)));
typedef __bf16 bf16x8 __attribute__((ext_vector_type(8)));
typedef float f32x2 __attribute__((ext_vector_type(2)));
typedef float f32x4 __attribute__((ext_vector_type(4)));
typedef float f32x16 __attribute__((ext_vector_type(16)));
typedef unsigned u32x2 __attribute__((ext_vector_type(2)));
typedef unsigned u32x4 __attribute__((ext_vector_type(4)));
#define LDS3 __attribute__((address_space(3)))
#define DEV __device__ __forceinline__

constexpr unsigned WS_PF = 1024, WS_AF = WS_PF + 2048, WS_KF = WS_AF + 2048, WS_OF = WS_KF + 2048;
constexpr unsigned WS_WP = 16384, WP_HEAD = 86016, WP_Q = 57344;
constexpr unsigned WS_KVP = WS_WP + 16 * WP_HEAD;
constexpr unsigned WS_ACT = WS_KVP + 16 * 4 * 16 * 1024;
constexpr unsigned WS_END = WS_ACT + 64 * 144 * 1024;
constexpr unsigned WS_DBG = WS_END;
constexpr unsigned SLOT = 16384;
constexpr unsigned L_PHI = 131072, L_VFR = L_PHI + 16384, L_ST = L_VFR + 8192, L_CS = L_ST + 2048;
constexpr unsigned LDS_BYTES = 163840;
#define LA 3
#define KP 16
#define OP 26
#define SPIN_MAX 2000000

template <int I, int N, class F> DEV void sfor(F&& f) {
  if constexpr (I < N) { f(std::integral_constant<int, I>{}); sfor<I + 1, N>(f); }
}
constexpr int cmin(int a, int b) { return a < b ? a : b; }
constexpr int cmax(int a, int b) { return a > b ? a : b; }

template <int ROLE, int CH> struct Led {
  static constexpr int pp = ROLE == 0 ? 8 : 4;
  static constexpr int nb1 = (ROLE == 0 && CH == 0) ? 2 : 0;
  static constexpr int pre = CH == 0 ? nb1 + 4 * pp : 0;
  static constexpr int G(int k) {
    if (CH != 0) return 0;
    if (k >= 1 && k <= 3) return pp;
    if (k == 5) return 8;
    if (k == 6) return 6;
    if (k == 7) return 7;
    if (ROLE != 0) return 0;
    if (k == 4) return 8;
    if (k >= 8 && k <= 13) return 4;
    if (k >= 15 && k <= 23) return 4;
    if (k == 24) return 2;
    return 0;
  }
  static constexpr int LX(int k) {
    if (ROLE != 0) return 0;
    return (k == KP ? 8 : 0) + (k == OP ? 9 : 0);
  }
  static constexpr int LAct(int k) {
    if (ROLE != 0) return 0;
    if (k == 13) return 2 * LA;
    if (k >= 14 && k - 14 + LA <= 13) return 2;
    return 0;
  }
  static constexpr int St(int k) {
    if (ROLE != 0) return 0;
    if (k == 7 || k == 8) return 2;
    if (CH == 1 && k == KP + 3) return 2;
    return 0;
  }
  static constexpr int cum(int k) { int s = pre; for (int i = 0; i < k; i++) s += G(i) + LX(i) + LAct(i) + St(i); return s; }
  static constexpr int need(int k) {
    if (CH == 0) {
      if (k <= 3) return nb1 + (k + 1) * pp;
      if (k <= 6) return cum(k - 3) + pp;
      if (k == 7) return cum(5) + 4;
      if (k == 8) return cum(5) + 8;
      if (k == 9) return cum(6) + 4;
      if (k == 10) return cum(6) + 6;
      if (k == 11) return ROLE == 0 ? cum(9) : cum(7) + 4;
      if (k == 12) return ROLE == 0 ? cum(9) : cum(7) + 7;
      if (ROLE != 0) return -1;
      if (k >= 16 && k <= 21) return cum(k - 8) + 4;
      if (k >= 22 && k <= 27) return cum(k - 7) + 4;
      if (k == 28) return cum(24) + 2;
      return -1;
    } else {
      if (k == 2 || k == 11 || k == 13) return cum(k);
      if (ROLE == 0 && k == KP + 5) return cum(KP + 4);
      return -1;
    }
  }
  static constexpr int barN(int k) { int n = need(k); return n < 0 ? 63 : cmin(63, cum(k) - n); }
  static constexpr int mid(int k) { return cum(k) + G(k) + LX(k) + LAct(k); }
  static constexpr int actEnd(int u) { return u < LA ? cum(13) + G(13) + LX(13) + 2 * (u + 1) : mid(14 + u - LA); }
  static constexpr int actN(int u) { return cmin(63, mid(14 + u) - actEnd(u)); }
  static constexpr int t4N() { return cmin(63, mid(KP + 2) - (cum(KP) + G(KP) + 8)); }
  static constexpr int oN() { return cmin(63, cum(28) - (cum(OP) + G(OP) + LX(OP))); }
};

DEV u32x4 mksrd(const void* p, unsigned bytes) {
  u64 a = (u64)p;
  u32x4 s; s[0] = (unsigned)a; s[1] = (unsigned)(a >> 32) & 0xffffu; s[2] = bytes; s[3] = 0x00020000u;
  return s;
}
template <bool SC1> DEV void glds(unsigned m0, unsigned voff, u32x4 srd, unsigned soff) {
  if constexpr (SC1)
    asm volatile("s_mov_b32 m0, %0\n\ts_nop 0\n\tbuffer_load_dwordx4 %1, %2, %3 offen sc1 lds" ::"s"(m0), "v"(voff), "s"(srd), "s"(soff) : "memory");
  else
    asm volatile("s_mov_b32 m0, %0\n\ts_nop 0\n\tbuffer_load_dwordx4 %1, %2, %3 offen lds" ::"s"(m0), "v"(voff), "s"(srd), "s"(soff) : "memory");
}
template <bool SC1, int OFF> DEV void vload(u32x4& d, unsigned voff, u32x4 srd, unsigned soff) {
  if constexpr (SC1)
    asm volatile("buffer_load_dwordx4 %0, %1, %2, %3 offen offset:%4 sc1" : "=v"(d) : "v"(voff), "s"(srd), "s"(soff), "n"(OFF) : "memory");
  else
    asm volatile("buffer_load_dwordx4 %0, %1, %2, %3 offen offset:%4" : "=v"(d) : "v"(voff), "s"(srd), "s"(soff), "n"(OFF) : "memory");
}
template <int OFF> DEV void vstore(u32x4 d, unsigned voff, u32x4 srd, unsigned soff) {
  asm volatile("buffer_store_dwordx4 %0, %1, %2, %3 offen offset:%4 sc1\n\ts_nop 1" ::"v"(d), "v"(voff), "s"(srd), "s"(soff), "n"(OFF) : "memory");
}
template <int N> DEV void bar() { asm volatile("s_waitcnt vmcnt(%0) lgkmcnt(0)\n\ts_barrier" ::"n"(N) : "memory"); }

DEV f32x16 mfma(u32x4 a, u32x4 b, f32x16 c) {
  return __builtin_amdgcn_mfma_f32_32x32x16_bf16(__builtin_bit_cast(bf16x8, a), __builtin_bit_cast(bf16x8, b), c, 0, 0, 0);
}
DEV float dot2(unsigned a, unsigned b, float c) {
  return __builtin_amdgcn_fdot2_f32_bf16(__builtin_bit_cast(bf16x2, a), __builtin_bit_cast(bf16x2, b), c, false);
}
DEV unsigned pk2(float lo, float hi) { f32x2 v = {lo, hi}; return __builtin_bit_cast(unsigned, __builtin_convertvector(v, bf16x2)); }
DEV float bflo(unsigned v) { return __uint_as_float(v << 16); }
DEV float bfhi(unsigned v) { return __uint_as_float(v & 0xffff0000u); }
DEV u32x4 ld128(LDS3 char* p) { return *(LDS3 u32x4*)p; }
DEV u32x4 ldtr(LDS3 char* p0, LDS3 char* p1) {
  bf16x4 a = __builtin_amdgcn_ds_read_tr16_b64_v4bf16((LDS3 bf16x4*)p0);
  bf16x4 b = __builtin_amdgcn_ds_read_tr16_b64_v4bf16((LDS3 bf16x4*)p1);
  return __builtin_bit_cast(u32x4, __builtin_shufflevector(a, b, 0, 1, 2, 3, 4, 5, 6, 7));
}
DEV u32x4 pack8(const f32x16& a, int o) {
  u32x4 r; r[0] = pk2(a[o], a[o + 1]); r[1] = pk2(a[o + 2], a[o + 3]); r[2] = pk2(a[o + 4], a[o + 5]); r[3] = pk2(a[o + 6], a[o + 7]);
  return r;
}
DEV float xhalf_max(float v) { auto s = __builtin_amdgcn_permlane32_swap(__float_as_uint(v), __float_as_uint(v), false, false); return fmaxf(__uint_as_float(s[0]), __uint_as_float(s[1])); }
DEV float xhalf_min(float v) { auto s = __builtin_amdgcn_permlane32_swap(__float_as_uint(v), __float_as_uint(v), false, false); return fminf(__uint_as_float(s[0]), __uint_as_float(s[1])); }
DEV float xhalf_add(float v) { auto s = __builtin_amdgcn_permlane32_swap(__float_as_uint(v), __float_as_uint(v), false, false); return __uint_as_float(s[0]) + __uint_as_float(s[1]); }
DEV u32x4 cfrag(float cs, unsigned e16) {
  unsigned hi = pk2(cs, cs);
  float lo = cs - bflo(hi);
  u32x4 r; r[0] = hi; r[1] = (pk2(lo, 0.f) & 0xffffu) | (e16 << 16); r[2] = e16; r[3] = 0;
  return r;
}
DEV float sum4(f32x4 v) { return (v[0] + v[1]) + (v[2] + v[3]); }

DEV void dual_softmax(const f32x16& a, float rl, float sc, bool hi_half, f32x16& p, f32x16& n) {
  float t[16];
#pragma unroll
  for (int v = 0; v < 16; v++) t[v] = (v >= 12 && hi_half) ? a[0] : a[v];
  float mx = t[0], mn = t[0];
#pragma unroll
  for (int v = 1; v < 16; v++) { mx = fmaxf(mx, t[v]); mn = fminf(mn, t[v]); }
  mx = xhalf_max(mx); mn = xhalf_min(mn);
  float c1 = -mx * rl, c2 = mn * rl, s1 = 0.f, s2 = 0.f;
#pragma unroll
  for (int v = 0; v < 16; v++) {
    float e1 = __builtin_amdgcn_exp2f(__builtin_fmaf(t[v], rl, c1));
    float e2 = __builtin_amdgcn_exp2f(__builtin_fmaf(-t[v], rl, c2));
    if (v >= 12) { e1 = hi_half ? 0.f : e1; e2 = hi_half ? 0.f : e2; }
    p[v] = e1; n[v] = e2; s1 += e1; s2 += e2;
  }
  s1 = sc * __builtin_amdgcn_rcpf(xhalf_add(s1));
  s2 = sc * __builtin_amdgcn_rcpf(xhalf_add(s2));
#pragma unroll
  for (int v = 0; v < 16; v++) { p[v] *= s1; n[v] *= s2; }
}

DEV void poll(const char* ws, unsigned off, u64 tag) {
  const u64* p = (const u64*)(ws + off);
#pragma unroll 1
  for (int it = 0; it < SPIN_MAX; it++) {
    const u64 v = __hip_atomic_load(p, __ATOMIC_RELAXED, __HIP_MEMORY_SCOPE_AGENT);
    if (__builtin_amdgcn_ballot_w64(v != tag) == 0) break;
    __builtin_amdgcn_s_sleep(1);
  }
}
DEV void flag(char* ws, unsigned off, u64 tag, int l) {
  if (l == 0) __hip_atomic_store((u64*)(ws + off), tag, __ATOMIC_RELAXED, __HIP_MEMORY_SCOPE_AGENT);
}

#define TS(k) do { unsigned long long t_ = __builtin_amdgcn_s_memrealtime(); asm volatile("v_writelane_b32 %0, %1, %2" : "+v"(stampv) : "s"((unsigned)t_), "n"(k)); } while (0)
#define TR(k) do { unsigned long long t_ = __builtin_amdgcn_s_memrealtime(); asm volatile("v_writelane_b32 %0, %1, %2" : "+v"(stampv) : "s"((unsigned)t_), "n"(k)); } while (0)

template <int ROLE, int CH>
DEV void body(const u16* __restrict__ x, const u16* __restrict__ wqkv, const u16* __restrict__ wphi, const u16* __restrict__ wo,
              const u16* __restrict__ w1, const u16* __restrict__ b1, const u16* __restrict__ w2, u16* __restrict__ out, char* ws,
              LDS3 char* lds, const int w, const int r, const int c, const int R, const int nt) {
  using L = Led<ROLE, CH>;
  const int l = threadIdx.x & 63, lr = l & 31, hh = l >> 5;
  const int g = w & 3;
  const int q4 = (l & 15) >> 2, p4 = l & 3, blk = (l >> 4) & 1;
  const unsigned ldsb = (unsigned)(size_t)lds;
  unsigned stampv = 0;
  TS(40); TR(42);
  const u32x4 sX = mksrd(x, 2048 * 896), sW1 = mksrd(w1, 448 * 3584), sW2 = mksrd(w2, 1792 * 896), sWO = mksrd(wo, 448 * 896);
  const u32x4 sWS = mksrd(ws, WS_END);
  const unsigned m0w = ldsb + g * 1024;
  const unsigned vX = (8 * g + (l >> 3)) * 896 + (((l & 7) ^ (((g & 1) << 2) | (l >> 4))) << 4);
  const unsigned vW1 = (4 * g + (l >> 4)) * 3584 + (((l & 15) ^ ((l >> 4) << 2)) << 4);
  const unsigned vW2 = (8 * g + (l >> 3)) * 896 + (((l & 7) ^ (((l >> 4) & 1) << 2)) << 4);
  const unsigned vLin = g * 1024 + l * 16;
  const unsigned l16 = l * 16;

  auto pageX = [&](int kc, int slot) {
#pragma unroll
    for (int j = 0; j < 4; j++) glds<false>(m0w + slot * SLOT + j * 4096, vX, sX, (128 * r + 32 * j) * 896 + kc * 128);
  };
  auto pageW1 = [&](int kc, int slot) {
#pragma unroll
    for (int j = 0; j < 4; j++) glds<false>(m0w + slot * SLOT + j * 4096, vW1, sW1, (64 * kc + 16 * j) * 3584 + c * 256);
  };
  auto pageW2 = [&](int u, int slot) {
#pragma unroll
    for (int j = 0; j < 4; j++) glds<false>(m0w + slot * SLOT + j * 4096, vW2, sW2, (128 * u + 32 * j) * 896 + nt * 128);
  };
  auto pageWO = [&](int u, int n) {
#pragma unroll
    for (int j = 0; j < n; j++) glds<false>(m0w + (4 + u) * SLOT + j * 4096, vW2, sWO, (128 * u + 32 * j) * 896 + nt * 128);
  };
  auto pageLin = [&](unsigned off, int slot, int n) {
#pragma unroll
    for (int j = 0; j < n; j++) glds<true>(m0w + slot * SLOT + j * 4096, vLin, sWS, WS_WP + c * WP_HEAD + off + j * 4096);
  };

  unsigned b1v[2] = {0, 0};
  u64 tag = 0;
  u32x4 pA0 = {0, 0, 0, 0}, pA1 = {0, 0, 0, 0}, pB0 = {0, 0, 0, 0}, pB1 = {0, 0, 0, 0};
  if constexpr (CH == 0) {
    if constexpr (ROLE == 0) {
      const u32x4 sB1 = mksrd(b1, 1792 * 2);
      const unsigned vb = (128 * c + lr) * 2;
      asm volatile("buffer_load_ushort %0, %1, %2, 0 offen" : "=v"(b1v[0]) : "v"(vb), "s"(sB1) : "memory");
      asm volatile("buffer_load_ushort %0, %1, %2, 0 offen offset:64" : "=v"(b1v[1]) : "v"(vb), "s"(sB1) : "memory");
    }
#pragma unroll
    for (int kc = 0; kc < 4; kc++) { pageX(kc, 2 * kc); if constexpr (ROLE == 0) pageW1(kc, 2 * kc + 1); }
  } else {
    if (w == 7) tag = __hip_atomic_load((const u64*)ws, __ATOMIC_RELAXED, __HIP_MEMORY_SCOPE_AGENT) + 1;
    if constexpr (ROLE == 0) {
      b1v[0] = b1[128 * c + 64 + lr]; b1v[1] = b1[128 * c + 96 + lr];
      if (w != 7) {
        const int kk = (lr & 0x13) | ((lr & 4) << 1) | ((lr & 8) >> 1);
        const char* a = (const char*)wqkv + (32 * c + kk) * 2688 + (g * 448 + 28 * r + 8 * hh) * 2;
        u32x2 a0 = *(const u32x2*)a, a1 = *(const u32x2*)(a + 8), a2 = *(const u32x2*)(a + 32), a3 = *(const u32x2*)(a + (hh ? 32 : 40));
        pA0 = u32x4{a0[0], a0[1], a1[0], a1[1]}; pA1 = u32x4{a2[0], a2[1], a3[0], a3[1]};
        if (g < 2) {
          __amdgpu_buffer_rsrc_t rp = __builtin_amdgcn_make_buffer_rsrc((void*)wphi, 0, 16 * 784 * 2, 0x00020000);
          unsigned e[16];
#pragma unroll
          for (int i = 0; i < 16; i++) {
            const int d = (i >> 3) * 16 + 8 * hh + (i & 7);
            const unsigned o = (d < 28 && lr < 28) ? (unsigned)(r * 784 + d * 28 + lr) * 2u : 0x40000000u;
            e[i] = __builtin_amdgcn_raw_buffer_load_b16(rp, o, 0, 0);
          }
#pragma unroll
          for (int i = 0; i < 4; i++) { pB0[i] = e[2 * i] | (e[2 * i + 1] << 16); pB1[i] = e[8 + 2 * i] | (e[9 + 2 * i] << 16); }
        } else {
#pragma unroll
          for (int i = 0; i < 4; i++) {
            const int d = 8 * hh + 2 * i;
            pB0[i] = (lr == d ? 0x3F80u : 0u) | (lr == d + 1 ? 0x3F800000u : 0u);
            pB1[i] = lr < 28 ? ((lr == d + 16 ? 0x3F80u : 0u) | (lr == d + 17 ? 0x3F800000u : 0u)) : 0u;
          }
        }
      }
    }
  }

  unsigned xa[4], wa[4][2], rot[4];
#pragma unroll
  for (int tt = 0; tt < 4; tt++) {
    const int s = (tt + g) & 3, row = 32 * g + lr;
    xa[tt] = row * 128 + (((2 * s + hh) ^ ((row >> 1) & 7)) << 4);
#pragma unroll
    for (int i = 0; i < 2; i++) wa[tt][i] = (16 * s + 8 * hh + q4) * 256 + ((((8 * CH + 4 * i) ^ (q4 << 2)) + 2 * blk + (p4 >> 1)) << 4) + 8 * (p4 & 1);
    rot[tt] = s;
  }

  u32x4 xf[28];
  f32x16 acc[2] = {{0}, {0}};
  float cs[2] = {0.f, 0.f}, st0 = 0.f, st1 = 0.f;
  const unsigned ONES = 0x3F803F80u;

  sfor<0, 7>([&](auto KC) {
    constexpr int kc = KC;
    if constexpr (CH == 1 && kc == 5) { if (w == 7) poll(ws, WS_PF + c * 128 + (l < 14 ? l : 0) * 8, tag); }
    bar<L::barN(kc)>();
    TS(kc);
    if constexpr (CH == 0 && ROLE == 0 && kc == 0) asm volatile("" : "+v"(b1v[0]), "+v"(b1v[1]));
    if constexpr (CH == 1 && ROLE == 0 && kc == 2) { if (w == 7) flag(ws, WS_PF + r * 128 + c * 8, tag, l); }
    constexpr unsigned sx = ((2 * kc) & 7) * SLOT, sw = sx + SLOT;
    u32x4 af[4][2];
    sfor<0, 4>([&](auto TT) {
      constexpr int tt = TT;
      xf[4 * kc + tt] = ld128(lds + xa[tt] + sx);
      if constexpr (ROLE == 0) {
        af[tt][0] = ldtr(lds + wa[tt][0] + sw, lds + wa[tt][0] + sw + 1024);
        af[tt][1] = ldtr(lds + wa[tt][1] + sw, lds + wa[tt][1] + sw + 1024);
      }
      if constexpr (tt == 0) {
        if constexpr (CH == 0) {
          if constexpr (kc >= 1 && kc <= 3) { pageX(kc + 3, (2 * kc + 6) & 7); if constexpr (ROLE == 0) pageW1(kc + 3, (2 * kc + 7) & 7); }
          if constexpr (kc == 4 && ROLE == 0) { pageW2(0, 6); pageW2(1, 7); }
          if constexpr (kc == 5) { pageLin(0, 0, 4); pageLin(16384, 1, 4); }
          if constexpr (kc == 6) { pageLin(32768, 2, 4); pageLin(49152, 3, 2); }
        } else if constexpr (ROLE == 0 && kc == 0) {
          asm volatile("" : "+v"(b1v[0]), "+v"(b1v[1]));
          if (w != 7) {
            f32x16 d = {0};
            d = mfma(pA0, pB0, d); d = mfma(pA1, pB1, d);
            const unsigned so = WS_WP + r * WP_HEAD + (g == 0 ? WP_Q + 2 * c * 1024 : 2 * c * 2048 + (g - 1) * 1024);
            vstore<0>(pack8(d, 0), l16, sWS, so);
            vstore<0>(pack8(d, 8), l16, sWS, so + (g == 0 ? 1024 : 2048));
          }
        }
      }
      const u32x4 xv = xf[4 * kc + tt];
      if constexpr (ROLE == 0) {
        acc[0] = mfma(af[tt][0], xv, acc[0]);
        acc[1] = mfma(af[tt][1], xv, acc[1]);
        if constexpr (tt == 0) {
#pragma unroll
          for (int i = 0; i < 4; i++) { cs[0] = dot2(af[0][0][i], ONES, cs[0]); cs[1] = dot2(af[0][1][i], ONES, cs[1]); }
        }
      }
      st0 = dot2(xv[0], CH == 0 ? ONES : xv[0], st0); st1 = dot2(xv[1], CH == 0 ? ONES : xv[1], st1);
      st0 = dot2(xv[2], CH == 0 ? ONES : xv[2], st0); st1 = dot2(xv[3], CH == 0 ? ONES : xv[3], st1);
    });
  });
  *(LDS3 float*)(lds + L_ST + CH * 1024 + (32 * g + lr) * 8 + hh * 4) = st0 + st1;
  if constexpr (ROLE == 0) {
    *(LDS3 float*)(lds + L_CS + (2 * CH) * 1024 + l16 + g * 4) = cs[0];
    *(LDS3 float*)(lds + L_CS + (2 * CH + 1) * 1024 + l16 + g * 4) = cs[1];
  }

  u32x4 mfrag;
  float rr, rl;
  f32x16 accB = {0};
  float csB = 0.f;
  const unsigned actv = (4 * r + g) * 144 * 1024 + l16;
  auto mish8 = [&](const f32x16& a, int o) {
    float t[8];
#pragma unroll
    for (int j = 0; j < 8; j++) {
      const float u = __builtin_amdgcn_exp2f(a[o + j] * rl);
      const float d = __builtin_fmaf(u, u + 2.f, 2.f);
      const float k = __builtin_fmaf(-2.f, __builtin_amdgcn_rcpf(d), 1.f);
      t[j] = (a[o + j] * rr) * k;
    }
    return u32x4{pk2(t[0], t[1]), pk2(t[2], t[3]), pk2(t[4], t[5]), pk2(t[6], t[7])};
  };
  sfor<0, 4>([&](auto V) {
    constexpr int v = V, k = 7 + v;
    bar<L::barN(k)>();
    TS(k);
    if constexpr (v == 0) {
      const f32x2 s1 = *(LDS3 f32x2*)(lds + L_ST + (32 * g + lr) * 8), s2 = *(LDS3 f32x2*)(lds + L_ST + 1024 + (32 * g + lr) * 8);
      const float m = (s1[0] + s1[1]) * (1.f / 448.f);
      const float var = fmaxf((s2[0] + s2[1]) * (1.f / 448.f) - m * m, 0.f) + 1e-5f;
      rr = __builtin_amdgcn_rsqf(var);
      const float sg = var * rr;
      rl = rr * 1.44269504088896f;
      const unsigned mh = pk2(-m, sg);
      const unsigned ml = pk2(-m - bflo(mh), sg - bfhi(mh));
      mfrag[0] = (mh & 0xffffu) | (ml << 16); mfrag[1] = mh; mfrag[2] = ml >> 16; mfrag[3] = 0;
      if constexpr (ROLE == 0) {
#pragma unroll
        for (int i = 0; i < 2; i++) {
          const float t = sum4(*(LDS3 f32x4*)(lds + L_CS + (2 * CH + i) * 1024 + l16));
          acc[i] = mfma(cfrag(t, hh ? 0u : b1v[i]), mfrag, acc[i]);
        }
      }
    }
    sfor<0, (v == 3 ? 4 : 8)>([&](auto S) {
      constexpr int s = S, kc = 2 * v + (s >> 2), tt = s & 3;
      const u32x4 f = ld128(lds + rot[tt] * 2048 + l16 + (CH * 1024 + v * SLOT + (kc & 1) * 8192));
      if constexpr (s == 0 && CH == 0) {
        if constexpr (v == 0) { pageLin(WP_Q, 4, 4); pageLin(WP_Q + 16384, 5, 3); }
        else if constexpr (ROLE == 0) pageW2(1 + v, v - 1);
      }
      if constexpr (CH == 0) accB = mfma(f, xf[4 * kc + tt], accB); else accB = mfma(xf[4 * kc + tt], f, accB);
      if constexpr (tt == 0) {
#pragma unroll
        for (int i = 0; i < 4; i++) csB = dot2(f[i], ONES, csB);
      }
    });
    if constexpr (ROLE == 0 && v < 2) {
      const unsigned so = WS_ACT + (8 * c + 4 * CH + 2 * v) * 1024;
      vstore<0>(mish8(acc[v], 0), actv, sWS, so);
      vstore<1024>(mish8(acc[v], 8), actv, sWS, so);
    }
  });
  *(LDS3 float*)(lds + L_CS + CH * 1024 + l16 + g * 4) = csB;

  bar<L::barN(11)>();
  TS(11);
  f32x16 accQ = {0};
  float csQ = 0.f;
  u32x4 phq[4];
  if constexpr (CH == 0) {
    const float t = sum4(*(LDS3 f32x4*)(lds + L_CS + l16));
    if constexpr (ROLE == 0) pageW2(5, 3);
    accB = mfma(cfrag(t, 0u), mfrag, accB);
    f32x16 p, n;
    dual_softmax(accB, rl, rr, hh != 0, p, n);
    const int T = 32 * g + lr;
    const unsigned gs = (T & 5) | ((T & 2) << 2) | ((T & 8) >> 2);
#pragma unroll
    for (int u = 0; u < 4; u++) {
      *(LDS3 u32x2*)(lds + L_PHI + T * 128 + (((2 * u + hh) ^ gs) << 3)) = u32x2{pk2(p[4 * u], p[4 * u + 1]), pk2(p[4 * u + 2], p[4 * u + 3])};
      *(LDS3 u32x2*)(lds + L_PHI + T * 128 + (((8 + 2 * u + hh) ^ gs) << 3)) = u32x2{pk2(n[4 * u], n[4 * u + 1]), pk2(n[4 * u + 2], n[4 * u + 3])};
    }
  } else {
    if constexpr (ROLE == 0) { if (w == 7) flag(ws, WS_AF + r * 128 + c * 8, tag, l); }
    const float t = sum4(*(LDS3 f32x4*)(lds + L_CS + 1024 + l16));
    accB = mfma(mfrag, cfrag(t, l == 28 ? 0x3F80u : 0u), accB);
    *(LDS3 u32x4*)(lds + L_VFR + 2 * g * 1024 + l16) = pack8(accB, 0);
    *(LDS3 u32x4*)(lds + L_VFR + (2 * g + 1) * 1024 + l16) = pack8(accB, 8);
    sfor<0, 16>([&](auto S) {
      constexpr int s = S, kc = s >> 2, tt = s & 3;
      const u32x4 f = ld128(lds + rot[tt] * 1024 + l16 + (4 * SLOT + kc * 4096));
      accQ = mfma(f, xf[4 * kc + tt], accQ);
      if constexpr (tt == 0) {
#pragma unroll
        for (int i = 0; i < 4; i++) csQ = dot2(f[i], ONES, csQ);
      }
    });
  }
  bar<L::barN(12)>();
  TS(12);
  if constexpr (CH == 0) {
    if constexpr (ROLE == 0) pageW2(6, 4);
  } else {
    if (g < 2) {
      f32x16 kv = {0};
      const int T0 = 4 * hh + q4;
      const unsigned gs0 = (T0 & 5) | ((T0 & 2) << 2);
      const unsigned a0 = L_PHI + T0 * 128 + (((8 * g + 4 * blk + p4) ^ gs0) << 3);
      const unsigned a1 = L_PHI + (T0 + 8) * 128 + (((8 * g + 4 * blk + p4) ^ gs0 ^ 2) << 3);
#pragma unroll
      for (int s = 0; s < 8; s++) kv = mfma(ldtr(lds + a0 + s * 2048, lds + a1 + s * 2048), ld128(lds + L_VFR + s * 1024 + l16), kv);
      const unsigned so = WS_KVP + ((c * 4 + 2 * g) * 16 + r) * 1024;
      vstore<0>(pack8(kv, 0), l16, sWS, so);
      vstore<0>(pack8(kv, 8), l16, sWS, so + 16384);
    }
    sfor<16, 28>([&](auto S) {
      constexpr int s = S, kc = s >> 2, tt = s & 3;
      const u32x4 f = ld128(lds + rot[tt] * 1024 + l16 + (5 * SLOT + (kc - 4) * 4096));
      accQ = mfma(f, xf[4 * kc + tt], accQ);
      if constexpr (tt == 0) {
#pragma unroll
        for (int i = 0; i < 4; i++) csQ = dot2(f[i], ONES, csQ);
      }
    });
    *(LDS3 float*)(lds + L_CS + 2048 + l16 + g * 4) = csQ;
    if constexpr (ROLE == 0) { if (w == 7) poll(ws, WS_AF + (R >> 1) * 128 + (l < 14 ? l : 0) * 8, tag); }
  }
  bar<L::barN(13)>();
  TS(13);
  u32x4 al[14][2];
  const unsigned actr = (2 * R + CH) * 144 * 1024 + 2 * g * 1024 + l16;
  if constexpr (ROLE == 0) {
    if constexpr (CH == 0) pageW2(7, 5);
    sfor<0, LA>([&](auto U) { constexpr int u = U; vload<true, 0>(al[u][0], actr, sWS, WS_ACT + u * 8192); vload<true, 1024>(al[u][1], actr, sWS, WS_ACT + u * 8192); });
  }
  if constexpr (CH == 1) {
    if (w == 7) flag(ws, WS_KF + c * 128 + r * 8, tag, l);
    const float t = sum4(*(LDS3 f32x4*)(lds + L_CS + 2048 + l16));
    accQ = mfma(cfrag(t, 0u), mfrag, accQ);
    f32x16 p, n;
    dual_softmax(accQ, rl, 1.f, hh != 0, p, n);
    phq[0] = pack8(p, 0); phq[1] = pack8(p, 8); phq[2] = pack8(n, 0); phq[3] = pack8(n, 8);
  }

  u32x4 t4[8];
  auto t4_load = [&]() {
    const unsigned so = WS_KVP + ((c * 4 + g) * 16 + 8 * CH) * 1024;
    vload<true, 0>(t4[0], l16, sWS, so); vload<true, 1024>(t4[1], l16, sWS, so); vload<true, 2048>(t4[2], l16, sWS, so); vload<true, 3072>(t4[3], l16, sWS, so);
    vload<true, 0>(t4[4], l16, sWS, so + 4096); vload<true, 1024>(t4[5], l16, sWS, so + 4096); vload<true, 2048>(t4[6], l16, sWS, so + 4096); vload<true, 3072>(t4[7], l16, sWS, so + 4096);
  };
  auto t4_sum = [&]() {
    f32x4 s0 = {0, 0, 0, 0}, s1 = {0, 0, 0, 0};
    unsigned KL = 0x00003F80u, KH = 0x3F800000u;
    asm volatile("" : "+s"(KL), "+s"(KH));
#pragma unroll
    for (int i = 0; i < 8; i++) {
      s0[0] = dot2(t4[i][0], KL, s0[0]); s0[1] = dot2(t4[i][0], KH, s0[1]);
      s0[2] = dot2(t4[i][1], KL, s0[2]); s0[3] = dot2(t4[i][1], KH, s0[3]);
      s1[0] = dot2(t4[i][2], KL, s1[0]); s1[1] = dot2(t4[i][2], KH, s1[1]);
      s1[2] = dot2(t4[i][3], KL, s1[2]); s1[3] = dot2(t4[i][3], KH, s1[3]);
    }
    *(LDS3 f32x4*)(lds + L_PHI + w * 2048 + l16) = s0;
    *(LDS3 f32x4*)(lds + L_PHI + w * 2048 + 1024 + l16) = s1;
  };
  auto t4_o = [&]() {
    f32x16 o = {0};
#pragma unroll
    for (int f = 0; f < 4; f++) {
      const f32x4 a0 = *(LDS3 f32x4*)(lds + L_PHI + f * 2048 + l16) + *(LDS3 f32x4*)(lds + L_PHI + (f + 4) * 2048 + l16);
      const f32x4 a1 = *(LDS3 f32x4*)(lds + L_PHI + f * 2048 + 1024 + l16) + *(LDS3 f32x4*)(lds + L_PHI + (f + 4) * 2048 + 1024 + l16);
      o = mfma(u32x4{pk2(a0[0], a0[1]), pk2(a0[2], a0[3]), pk2(a1[0], a1[1]), pk2(a1[2], a1[3])}, phq[f], o);
    }
    auto sw = __builtin_amdgcn_permlane32_swap(__float_as_uint(o[12]), __float_as_uint(o[12]), false, false);
    const float rc = __builtin_amdgcn_rcpf(__uint_as_float(sw[1]) + 1e-6f);
#pragma unroll
    for (int v = 0; v < 16; v++) o[v] = (v >= 12 && hh) ? 0.f : o[v] * rc;
    const unsigned so = WS_ACT + (112 + 2 * c) * 1024;
    vstore<0>(pack8(o, 0), actv, sWS, so);
    vstore<1024>(pack8(o, 8), actv, sWS, so);
  };

  if constexpr (ROLE == 1) {
    if constexpr (CH == 1) { if (w == 7) poll(ws, WS_KF + c * 128 + (l & 15) * 8, tag); }
    bar<63>();
    TS(14);
    t4_load();
    asm volatile("s_waitcnt vmcnt(0)" : "+v"(t4[0]), "+v"(t4[1]), "+v"(t4[2]), "+v"(t4[3]), "+v"(t4[4]), "+v"(t4[5]), "+v"(t4[6]), "+v"(t4[7]));
    t4_sum();
    bar<63>();
    TS(15);
    if constexpr (CH == 1) t4_o();
    bar<0>();
    TS(16);
    if constexpr (CH == 1) {
      if (w == 7) {
        flag(ws, WS_OF + r * 128 + c * 8, tag, l);
        if (r == 0 && c == 15) {
#pragma unroll
          for (int i = 0; i < 4; i++) poll(ws, WS_AF + i * 512 + ((l & 15) < 14 ? l : 0) * 8, tag);
          poll(ws, WS_KF + 14 * 128 + (l & 31) * 8, tag);
          flag(ws, 0, tag, l);
        }
      }
    }
  } else {
    f32x16 oa[2] = {{0}, {0}};
    unsigned ua[2];
#pragma unroll
    for (int i = 0; i < 2; i++) ua[i] = (4 * hh + q4) * 128 + ((((4 * i) ^ ((q4 >> 1) << 2)) + 2 * blk + (p4 >> 1)) << 4) + 8 * (p4 & 1);
    u32x4 of[8], xr;
    sfor<0, 14>([&](auto U) {
      constexpr int u = U, k = 14 + u;
      if constexpr (CH == 1 && k == KP) { if (w == 7) poll(ws, WS_KF + c * 128 + (l & 15) * 8, tag); }
      if constexpr (CH == 1 && k == OP) { if (w == 7) poll(ws, WS_OF + (R >> 1) * 128 + (l & 15) * 8, tag); }
      bar<L::barN(k)>();
      TS(k);
      if constexpr (CH == 1 && k == KP + 5) { if (w == 7) flag(ws, WS_OF + r * 128 + c * 8, tag, l); }
      constexpr unsigned su = ((u + 6) & 7) * SLOT;
      u32x4 a[2][2];
#pragma unroll
      for (int e = 0; e < 2; e++)
#pragma unroll
        for (int i = 0; i < 2; i++) a[e][i] = ldtr(lds + ua[i] + g * 4096 + (su + e * 2048), lds + ua[i] + g * 4096 + (su + e * 2048 + 1024));
      if constexpr (CH == 0) {
        if constexpr (u >= 1 && u <= 6) pageW2(7 + u, (u + 5) & 7);
        if constexpr (u >= 7 && u <= 10) pageWO(u - 7, u == 10 ? 2 : 4);
      }
      if constexpr (k == KP) t4_load();
      if constexpr (k == OP) {
        const unsigned so = WS_ACT + 112 * 1024;
        sfor<0, 4>([&](auto J) { constexpr int jj = J; vload<true, 0>(of[2 * jj], actr, sWS, so + jj * 8192); vload<true, 1024>(of[2 * jj + 1], actr, sWS, so + jj * 8192); });
        vload<false, 0>(xr, (64 * R + (threadIdx.x >> 3)) * 896 + (threadIdx.x & 7) * 16, sX, nt * 128);
      }
      if constexpr (u + LA <= 13) { vload<true, 0>(al[u + LA][0], actr, sWS, WS_ACT + (u + LA) * 8192); vload<true, 1024>(al[u + LA][1], actr, sWS, WS_ACT + (u + LA) * 8192); }
      asm volatile("s_waitcnt vmcnt(%2)" : "+v"(al[u][0]), "+v"(al[u][1]) : "n"(L::actN(u)));
#pragma unroll
      for (int e = 0; e < 2; e++)
#pragma unroll
        for (int i = 0; i < 2; i++) oa[i] = mfma(a[e][i], al[u][e], oa[i]);
      if constexpr (k == KP + 2) {
        asm volatile("s_waitcnt vmcnt(%8)" : "+v"(t4[0]), "+v"(t4[1]), "+v"(t4[2]), "+v"(t4[3]), "+v"(t4[4]), "+v"(t4[5]), "+v"(t4[6]), "+v"(t4[7]) : "n"(L::t4N()));
        t4_sum();
      }
      if constexpr (k == KP + 3 && CH == 1) t4_o();
    });
    bar<L::barN(28)>();
    TS(28);
    asm volatile("s_waitcnt vmcnt(%9)" : "+v"(of[0]), "+v"(of[1]), "+v"(of[2]), "+v"(of[3]), "+v"(of[4]), "+v"(of[5]), "+v"(of[6]), "+v"(of[7]), "+v"(xr) : "n"(L::oN()));
#pragma unroll
    for (int jj = 0; jj < 4; jj++)
#pragma unroll
      for (int t = 0; t < 2; t++)
#pragma unroll
        for (int i = 0; i < 2; i++) {
          LDS3 char* pb = lds + ua[i] + g * 3584 + (4 * SLOT + jj * 14336 + t * 2048);
          oa[i] = mfma(ldtr(pb, t == 0 ? pb + 1024 : pb + 1024 - hh * 512), of[2 * jj + t], oa[i]);
        }
    {
      const int T = 32 * CH + lr;
#pragma unroll
      for (int i = 0; i < 2; i++)
#pragma unroll
        for (int u = 0; u < 4; u++)
          *(LDS3 f32x4*)(lds + g * SLOT + T * 256 + (((8 * i + 2 * u + hh) ^ (T & 7)) << 4)) = f32x4{oa[i][4 * u], oa[i][4 * u + 1], oa[i][4 * u + 2], oa[i][4 * u + 3]};
    }
    bar<63>();
    TS(29);
    {
      const int T = threadIdx.x >> 3, ck = threadIdx.x & 7;
      f32x4 s0 = {0, 0, 0, 0}, s1 = {0, 0, 0, 0};
#pragma unroll
      for (int qq = 0; qq < 4; qq++) {
        s0 += *(LDS3 f32x4*)(lds + qq * SLOT + T * 256 + (((2 * ck) ^ (T & 7)) << 4));
        s1 += *(LDS3 f32x4*)(lds + qq * SLOT + T * 256 + (((2 * ck + 1) ^ (T & 7)) << 4));
      }
      u32x4 o;
      o[0] = pk2(s0[0] + bflo(xr[0]), s0[1] + bfhi(xr[0])); o[1] = pk2(s0[2] + bflo(xr[1]), s0[3] + bfhi(xr[1]));
      o[2] = pk2(s1[0] + bflo(xr[2]), s1[1] + bfhi(xr[2])); o[3] = pk2(s1[2] + bflo(xr[3]), s1[3] + bfhi(xr[3]));
      *(u32x4*)((char*)out + (64 * R + T) * 896 + nt * 128 + ck * 16) = o;
    }
  }
  TS(41); TR(43);
  if (blockIdx.x == 16 && w == 0) {
    const unsigned d = 8u * (__builtin_amdgcn_readlane(stampv, 40) - __builtin_amdgcn_readlane(stampv, 40));
    const unsigned t0 = (unsigned)__builtin_amdgcn_s_memrealtime();
    for (int it = 0; it < 1000000; it++) { if ((unsigned)__builtin_amdgcn_s_memrealtime() - t0 >= d) break; __builtin_amdgcn_s_sleep(2); }
  }
}

__global__ __launch_bounds__(512, 2) void fused_kernel(const u16* __restrict__ x, const u16* __restrict__ wqkv, const u16* __restrict__ wphi,
                                                       const u16* __restrict__ wo, const u16* __restrict__ w1, const u16* __restrict__ b1,
                                                       const u16* __restrict__ w2, u16* __restrict__ out, char* ws) {
  __shared__ __attribute__((aligned(16))) char lds_[LDS_BYTES];
  LDS3 char* lds = (LDS3 char*)lds_;
  const int w = __builtin_amdgcn_readfirstlane(threadIdx.x >> 6);
  const int b = blockIdx.x, xcd = b & 7, j = b >> 3, p = xcd >> 1;
  const int r = 4 * p + (j >> 3), c = 8 * (xcd & 1) + (j & 7);
  int R, nt;
  if ((xcd & 1) == 0) { R = 8 * p + (j >> 2); nt = j & 3; }
  else { const int m = (j >> 3) * 6 + (j & 7); R = 8 * p + m / 3; nt = 4 + m % 3; }
  if (c < 14) {
    if (w < 4) body<0, 0>(x, wqkv, wphi, wo, w1, b1, w2, out, ws, lds, w, r, c, R, nt);
    else body<0, 1>(x, wqkv, wphi, wo, w1, b1, w2, out, ws, lds, w, r, c, R, nt);
  } else {
    if (w < 4) body<1, 0>(x, wqkv, wphi, wo, w1, b1, w2, out, ws, lds, w, r, c, R, nt);
    else body<1, 1>(x, wqkv, wphi, wo, w1, b1, w2, out, ws, lds, w, r, c, R, nt);
  }
}

extern "C" void kernel_launch(void* const* d_in, const int* in_sizes, int n_in, void* d_out, int out_size, void* d_ws, size_t ws_size,
                              hipStream_t stream) {
  fused_kernel<<<256, 512, 0, stream>>>((const u16*)d_in[0], (const u16*)d_in[1], (const u16*)d_in[2], (const u16*)d_in[3], (const u16*)d_in[5],
                                        (const u16*)d_in[6], (const u16*)d_in[7], (u16*)d_out, (char*)d_ws);
}
```

```cpp
#include <hip/hip_runtime.h>
#include <stdint.h>
#include <type_traits>
#define STAMP 1

typedef unsigned short u16;
typedef unsigned long long u64;
typedef __bf16 bf16x2 __attribute__((ext_vector_type(2)));
typedef __bf16 bf16x4 __attribute__((ext_vector_type(4)));
typedef __bf16 bf16x8 __attribute__((ext_vector_type(8)));
typedef float f32x2 __attribute__((ext_vector_type(2)));
typedef float f32x4 __attribute__((ext_vector_type(4)));
typedef float f32x16 __attribute__((ext_vector_type(16)));
typedef unsigned u32x2 __attribute__((ext_vector_type(2)));
typedef unsigned u32x4 __attribute__((ext_vector_type(4)));
#define LDS3 __attribute__((address_space(3)))
#define DEV __device__ __forceinline__

constexpr unsigned WS_PF = 1024, WS_AF = WS_PF + 2048, WS_KF = WS_AF + 2048, WS_OF = WS_KF + 2048;
constexpr unsigned WS_WP = 16384, WP_HEAD = 86016, WP_Q = 57344;
constexpr unsigned WS_KVP = WS_WP + 16 * WP_HEAD;
constexpr unsigned WS_ACT = WS_KVP + 16 * 4 * 16 * 1024;
constexpr unsigned WS_END = WS_ACT + 64 * 144 * 1024;
constexpr unsigned WS_DBG = WS_END;
constexpr unsigned SLOT = 16384;
constexpr unsigned L_PHI = 131072, L_VFR = L_PHI + 16384, L_ST = L_VFR + 8192, L_CS = L_ST + 2048;
constexpr unsigned LDS_BYTES = 163840;
#define LA 3
#define KP 16
#define OP 26
#define SPIN_MAX 2000000

template <int I, int N, class F> DEV void sfor(F&& f) {
  if constexpr (I < N) { f(std::integral_constant<int, I>{}); sfor<I + 1, N>(f); }
}
constexpr int cmin(int a, int b) { return a < b ? a : b; }
constexpr int cmax(int a, int b) { return a > b ? a : b; }

template <int ROLE, int CH> struct Led {
  static constexpr int pp = ROLE == 0 ? 8 : 4;
  static constexpr int nb1 = (ROLE == 0 && CH == 0) ? 2 : 0;
  static constexpr int pre = CH == 0 ? nb1 + 4 * pp : 0;
  static constexpr int G(int k) {
    if (CH != 0) return 0;
    if (k >= 1 && k <= 3) return pp;
    if (k == 5) return 8;
    if (k == 6) return 6;
    if (k == 7) return 7;
    if (ROLE != 0) return 0;
    if (k == 4) return 8;
    if (k >= 8 && k <= 13) return 4;
    if (k >= 15 && k <= 23) return 4;
    if (k == 24) return 2;
    return 0;
  }
  static constexpr int LX(int k) {
    if (ROLE != 0) return 0;
    return (k == KP ? 8 : 0) + (k == OP ? 9 : 0);
  }
  static constexpr int LAct(int k) {
    if (ROLE != 0) return 0;
    if (k == 13) return 2 * LA;
    if (k >= 14 && k - 14 + LA <= 13) return 2;
    return 0;
  }
  static constexpr int St(int k) {
    if (ROLE != 0) return 0;
    if (k == 7 || k == 8) return 2;
    if (CH == 1 && k == KP + 3) return 2;
    return 0;
  }
  static constexpr int cum(int k) { int s = pre; for (int i = 0; i < k; i++) s += G(i) + LX(i) + LAct(i) + St(i); return s; }
  static constexpr int need(int k) {
    if (CH == 0) {
      if (k <= 3) return nb1 + (k + 1) * pp;
      if (k <= 6) return cum(k - 3) + pp;
      if (k == 7) return cum(5) + 4;
      if (k == 8) return cum(5) + 8;
      if (k == 9) return cum(6) + 4;
      if (k == 10) return cum(6) + 6;
      if (k == 11) return ROLE == 0 ? cum(9) : cum(7) + 4;
      if (k == 12) return ROLE == 0 ? cum(9) : cum(7) + 7;
      if (ROLE != 0) return -1;
      if (k >= 16 && k <= 21) return cum(k - 8) + 4;
      if (k >= 22 && k <= 27) return cum(k - 7) + 4;
      if (k == 28) return cum(24) + 2;
      return -1;
    } else {
      if (k == 2 || k == 11 || k == 13) return cum(k);
      if (ROLE == 0 && k == KP + 5) return cum(KP + 4);
      return -1;
    }
  }
  static constexpr int barN(int k) { int n = need(k); return n < 0 ? 63 : cmin(63, cum(k) - n); }
  static constexpr int mid(int k) { return cum(k) + G(k) + LX(k) + LAct(k); }
  static constexpr int actEnd(int u) { return u < LA ? cum(13) + G(13) + LX(13) + 2 * (u + 1) : mid(14 + u - LA); }
  static constexpr int actN(int u) { return cmin(63, mid(14 + u) - actEnd(u)); }
  static constexpr int t4N() { return cmin(63, mid(KP + 2) - (cum(KP) + G(KP) + 8)); }
  static constexpr int oN() { return cmin(63, cum(28) - (cum(OP) + G(OP) + LX(OP))); }
};

DEV u32x4 mksrd(const void* p, unsigned bytes) {
  u64 a = (u64)p;
  u32x4 s; s[0] = (unsigned)a; s[1] = (unsigned)(a >> 32) & 0xffffu; s[2] = bytes; s[3] = 0x00020000u;
  return s;
}
template <bool SC1> DEV void glds(unsigned m0, unsigned voff, u32x4 srd, unsigned soff) {
  if constexpr (SC1)
    asm volatile("s_mov_b32 m0, %0\n\ts_nop 0\n\tbuffer_load_dwordx4 %1, %2, %3 offen sc1 lds" ::"s"(m0), "v"(voff), "s"(srd), "s"(soff) : "memory");
  else
    asm volatile("s_mov_b32 m0, %0\n\ts_nop 0\n\tbuffer_load_dwordx4 %1, %2, %3 offen lds" ::"s"(m0), "v"(voff), "s"(srd), "s"(soff) : "memory");
}
template <bool SC1, int OFF> DEV void vload(u32x4& d, unsigned voff, u32x4 srd, unsigned soff) {
  if constexpr (SC1)
    asm volatile("buffer_load_dwordx4 %0, %1, %2, %3 offen offset:%4 sc1" : "=v"(d) : "v"(voff), "s"(srd), "s"(soff), "n"(OFF) : "memory");
  else
    asm volatile("buffer_load_dwordx4 %0, %1, %2, %3 offen offset:%4" : "=v"(d) : "v"(voff), "s"(srd), "s"(soff), "n"(OFF) : "memory");
}
template <int OFF> DEV void vstore(u32x4 d, unsigned voff, u32x4 srd, unsigned soff) {
  asm volatile("buffer_store_dwordx4 %0, %1, %2, %3 offen offset:%4 sc1\n\ts_nop 1" ::"v"(d), "v"(voff), "s"(srd), "s"(soff), "n"(OFF) : "memory");
}
template <int N> DEV void bar() { asm volatile("s_waitcnt vmcnt(%0) lgkmcnt(0)\n\ts_barrier" ::"n"(N) : "memory"); }

DEV f32x16 mfma(u32x4 a, u32x4 b, f32x16 c) {
  return __builtin_amdgcn_mfma_f32_32x32x16_bf16(__builtin_bit_cast(bf16x8, a), __builtin_bit_cast(bf16x8, b), c, 0, 0, 0);
}
DEV float dot2(unsigned a, unsigned b, float c) {
  return __builtin_amdgcn_fdot2_f32_bf16(__builtin_bit_cast(bf16x2, a), __builtin_bit_cast(bf16x2, b), c, false);
}
DEV unsigned pk2(float lo, float hi) { f32x2 v = {lo, hi}; return __builtin_bit_cast(unsigned, __builtin_convertvector(v, bf16x2)); }
DEV float bflo(unsigned v) { return __uint_as_float(v << 16); }
DEV float bfhi(unsigned v) { return __uint_as_float(v & 0xffff0000u); }
DEV u32x4 ld128(LDS3 char* p) { return *(LDS3 u32x4*)p; }
DEV u32x4 ldtr(LDS3 char* p0, LDS3 char* p1) {
  bf16x4 a = __builtin_amdgcn_ds_read_tr16_b64_v4bf16((LDS3 bf16x4*)p0);
  bf16x4 b = __builtin_amdgcn_ds_read_tr16_b64_v4bf16((LDS3 bf16x4*)p1);
  return __builtin_bit_cast(u32x4, __builtin_shufflevector(a, b, 0, 1, 2, 3, 4, 5, 6, 7));
}
DEV u32x4 pack8(const f32x16& a, int o) {
  u32x4 r; r[0] = pk2(a[o], a[o + 1]); r[1] = pk2(a[o + 2], a[o + 3]); r[2] = pk2(a[o + 4], a[o + 5]); r[3] = pk2(a[o + 6], a[o + 7]);
  return r;
}
DEV float xhalf_max(float v) { auto s = __builtin_amdgcn_permlane32_swap(__float_as_uint(v), __float_as_uint(v), false, false); return fmaxf(__uint_as_float(s[0]), __uint_as_float(s[1])); }
DEV float xhalf_min(float v) { auto s = __builtin_amdgcn_permlane32_swap(__float_as_uint(v), __float_as_uint(v), false, false); return fminf(__uint_as_float(s[0]), __uint_as_float(s[1])); }
DEV float xhalf_add(float v) { auto s = __builtin_amdgcn_permlane32_swap(__float_as_uint(v), __float_as_uint(v), false, false); return __uint_as_float(s[0]) + __uint_as_float(s[1]); }
DEV u32x4 cfrag(float cs, unsigned e16) {
  unsigned hi = pk2(cs, cs);
  float lo = cs - bflo(hi);
  u32x4 r; r[0] = hi; r[1] = (pk2(lo, 0.f) & 0xffffu) | (e16 << 16); r[2] = e16; r[3] = 0;
  return r;
}
DEV float sum4(f32x4 v) { return (v[0] + v[1]) + (v[2] + v[3]); }

DEV void dual_softmax(const f32x16& a, float rl, float sc, bool hi_half, f32x16& p, f32x16& n) {
  float t[16];
#pragma unroll
  for (int v = 0; v < 16; v++) t[v] = (v >= 12 && hi_half) ? a[0] : a[v];
  float mx = t[0], mn = t[0];
#pragma unroll
  for (int v = 1; v < 16; v++) { mx = fmaxf(mx, t[v]); mn = fminf(mn, t[v]); }
  mx = xhalf_max(mx); mn = xhalf_min(mn);
  float c1 = -mx * rl, c2 = mn * rl, s1 = 0.f, s2 = 0.f;
#pragma unroll
  for (int v = 0; v < 16; v++) {
    float e1 = __builtin_amdgcn_exp2f(__builtin_fmaf(t[v], rl, c1));
    float e2 = __builtin_amdgcn_exp2f(__builtin_fmaf(-t[v], rl, c2));
    if (v >= 12) { e1 = hi_half ? 0.f : e1; e2 = hi_half ? 0.f : e2; }
    p[v] = e1; n[v] = e2; s1 += e1; s2 += e2;
  }
  s1 = sc * __builtin_amdgcn_rcpf(xhalf_add(s1));
  s2 = sc * __builtin_amdgcn_rcpf(xhalf_add(s2));
#pragma unroll
  for (int v = 0; v < 16; v++) { p[v] *= s1; n[v] *= s2; }
}

DEV void poll(const char* ws, unsigned off, u64 tag) {
  const u64* p = (const u64*)(ws + off);
#pragma unroll 1
  for (int it = 0; it < SPIN_MAX; it++) {
    const u64 v = __hip_atomic_load(p, __ATOMIC_RELAXED, __HIP_MEMORY_SCOPE_AGENT);
    if (__builtin_amdgcn_ballot_w64(v != tag) == 0) break;
    __builtin_amdgcn_s_sleep(1);
  }
}
DEV void flag(char* ws, unsigned off, u64 tag, int l) {
  if (l == 0) __hip_atomic_store((u64*)(ws + off), tag, __ATOMIC_RELAXED, __HIP_MEMORY_SCOPE_AGENT);
}

#define TS(k) do { unsigned long long t_ = __builtin_amdgcn_s_memrealtime(); asm volatile("v_writelane_b32 %0, %1, %2" : "+v"(stampv) : "s"((unsigned)t_), "n"(k)); } while (0)
#define TR(k) do { unsigned long long t_ = __builtin_amdgcn_s_memrealtime(); asm volatile("v_writelane_b32 %0, %1, %2" : "+v"(stampv) : "s"((unsigned)t_), "n"(k)); } while (0)

template <int ROLE, int CH>
DEV void body(const u16* __restrict__ x, const u16* __restrict__ wqkv, const u16* __restrict__ wphi, const u16* __restrict__ wo,
              const u16* __restrict__ w1, const u16* __restrict__ b1, const u16* __restrict__ w2, u16* __restrict__ out, char* ws,
              LDS3 char* lds, const int w, const int r, const int c, const int R, const int nt, const unsigned ttop) {
  using L = Led<ROLE, CH>;
  const int l = threadIdx.x & 63, lr = l & 31, hh = l >> 5;
  const int g = w & 3;
  const int q4 = (l & 15) >> 2, p4 = l & 3, blk = (l >> 4) & 1;
  const unsigned ldsb = (unsigned)(size_t)lds;
  unsigned stampv = 0;
  TS(40); TR(42);
  const u32x4 sX = mksrd(x, 2048 * 896), sW1 = mksrd(w1, 448 * 3584), sW2 = mksrd(w2, 1792 * 896), sWO = mksrd(wo, 448 * 896);
  const u32x4 sWS = mksrd(ws, WS_END);
  const unsigned m0w = ldsb + g * 1024;
  const unsigned vX = (8 * g + (l >> 3)) * 896 + (((l & 7) ^ (((g & 1) << 2) | (l >> 4))) << 4);
  const unsigned vW1 = (4 * g + (l >> 4)) * 3584 + (((l & 15) ^ ((l >> 4) << 2)) << 4);
  const unsigned vW2 = (8 * g + (l >> 3)) * 896 + (((l & 7) ^ (((l >> 4) & 1) << 2)) << 4);
  const unsigned vLin = g * 1024 + l * 16;
  const unsigned l16 = l * 16;

  auto pageX = [&](int kc, int slot) {
#pragma unroll
    for (int j = 0; j < 4; j++) glds<false>(m0w + slot * SLOT + j * 4096, vX, sX, (128 * r + 32 * j) * 896 + kc * 128);
  };
  auto pageW1 = [&](int kc, int slot) {
#pragma unroll
    for (int j = 0; j < 4; j++) glds<false>(m0w + slot * SLOT + j * 4096, vW1, sW1, (64 * kc + 16 * j) * 3584 + c * 256);
  };
  auto pageW2 = [&](int u, int slot) {
#pragma unroll
    for (int j = 0; j < 4; j++) glds<false>(m0w + slot * SLOT + j * 4096, vW2, sW2, (128 * u + 32 * j) * 896 + nt * 128);
  };
  auto pageWO = [&](int u, int n) {
#pragma unroll
    for (int j = 0; j < n; j++) glds<false>(m0w + (4 + u) * SLOT + j * 4096, vW2, sWO, (128 * u + 32 * j) * 896 + nt * 128);
  };
  auto pageLin = [&](unsigned off, int slot, int n) {
#pragma unroll
    for (int j = 0; j < n; j++) glds<true>(m0w + slot * SLOT + j * 4096, vLin, sWS, WS_WP + c * WP_HEAD + off + j * 4096);
  };

  unsigned b1v[2] = {0, 0};
  u64 tag = 0;
  u32x4 pA0 = {0, 0, 0, 0}, pA1 = {0, 0, 0, 0}, pB0 = {0, 0, 0, 0}, pB1 = {0, 0, 0, 0};
  if constexpr (CH == 0) {
    if constexpr (ROLE == 0) {
      const u32x4 sB1 = mksrd(b1, 1792 * 2);
      const unsigned vb = (128 * c + lr) * 2;
      asm volatile("buffer_load_ushort %0, %1, %2, 0 offen" : "=v"(b1v[0]) : "v"(vb), "s"(sB1) : "memory");
      asm volatile("buffer_load_ushort %0, %1, %2, 0 offen offset:64" : "=v"(b1v[1]) : "v"(vb), "s"(sB1) : "memory");
    }
#pragma unroll
    for (int kc = 0; kc < 4; kc++) { pageX(kc, 2 * kc); if constexpr (ROLE == 0) pageW1(kc, 2 * kc + 1); }
  } else {
    if (w == 7) tag = __hip_atomic_load((const u64*)ws, __ATOMIC_RELAXED, __HIP_MEMORY_SCOPE_AGENT) + 1;
    if constexpr (ROLE == 0) {
      b1v[0] = b1[128 * c + 64 + lr]; b1v[1] = b1[128 * c + 96 + lr];
      if (w != 7) {
        const int kk = (lr & 0x13) | ((lr & 4) << 1) | ((lr & 8) >> 1);
        const char* a = (const char*)wqkv + (32 * c + kk) * 2688 + (g * 448 + 28 * r + 8 * hh) * 2;
        u32x2 a0 = *(const u32x2*)a, a1 = *(const u32x2*)(a + 8), a2 = *(const u32x2*)(a + 32), a3 = *(const u32x2*)(a + (hh ? 32 : 40));
        pA0 = u32x4{a0[0], a0[1], a1[0], a1[1]}; pA1 = u32x4{a2[0], a2[1], a3[0], a3[1]};
        if (g < 2) {
          __amdgpu_buffer_rsrc_t rp = __builtin_amdgcn_make_buffer_rsrc((void*)wphi, 0, 16 * 784 * 2, 0x00020000);
          unsigned e[16];
#pragma unroll
          for (int i = 0; i < 16; i++) {
            const int d = (i >> 3) * 16 + 8 * hh + (i & 7);
            const unsigned o = (d < 28 && lr < 28) ? (unsigned)(r * 784 + d * 28 + lr) * 2u : 0x40000000u;
            e[i] = __builtin_amdgcn_raw_buffer_load_b16(rp, o, 0, 0);
          }
#pragma unroll
          for (int i = 0; i < 4; i++) { pB0[i] = e[2 * i] | (e[2 * i + 1] << 16); pB1[i] = e[8 + 2 * i] | (e[9 + 2 * i] << 16); }
        } else {
#pragma unroll
          for (int i = 0; i < 4; i++) {
            const int d = 8 * hh + 2 * i;
            pB0[i] = (lr == d ? 0x3F80u : 0u) | (lr == d + 1 ? 0x3F800000u : 0u);
            pB1[i] = lr < 28 ? ((lr == d + 16 ? 0x3F80u : 0u) | (lr == d + 17 ? 0x3F800000u : 0u)) : 0u;
          }
        }
      }
    }
  }

  unsigned xa[4], wa[4][2], rot[4];
#pragma unroll
  for (int tt = 0; tt < 4; tt++) {
    const int s = (tt + g) & 3, row = 32 * g + lr;
    xa[tt] = row * 128 + (((2 * s + hh) ^ ((row >> 1) & 7)) << 4);
#pragma unroll
    for (int i = 0; i < 2; i++) wa[tt][i] = (16 * s + 8 * hh + q4) * 256 + ((((8 * CH + 4 * i) ^ (q4 << 2)) + 2 * blk + (p4 >> 1)) << 4) + 8 * (p4 & 1);
    rot[tt] = s;
  }

  u32x4 xf[28];
  f32x16 acc[2] = {{0}, {0}};
  float cs[2] = {0.f, 0.f}, st0 = 0.f, st1 = 0.f;
  const unsigned ONES = 0x3F803F80u;

  sfor<0, 7>([&](auto KC) {
    constexpr int kc = KC;
    if constexpr (CH == 1 && kc == 5) { if (w == 7) poll(ws, WS_PF + c * 128 + (l < 14 ? l : 0) * 8, tag); }
    bar<L::barN(kc)>();
    TS(kc);
    if constexpr (CH == 0 && ROLE == 0 && kc == 0) asm volatile("" : "+v"(b1v[0]), "+v"(b1v[1]));
    if constexpr (CH == 1 && ROLE == 0 && kc == 2) { if (w == 7) flag(ws, WS_PF + r * 128 + c * 8, tag, l); }
    constexpr unsigned sx = ((2 * kc) & 7) * SLOT, sw = sx + SLOT;
    u32x4 af[4][2];
    sfor<0, 4>([&](auto TT) {
      constexpr int tt = TT;
      xf[4 * kc + tt] = ld128(lds + xa[tt] + sx);
      if constexpr (ROLE == 0) {
        af[tt][0] = ldtr(lds + wa[tt][0] + sw, lds + wa[tt][0] + sw + 1024);
        af[tt][1] = ldtr(lds + wa[tt][1] + sw, lds + wa[tt][1] + sw + 1024);
      }
      if constexpr (tt == 0) {
        if constexpr (CH == 0) {
          if constexpr (kc >= 1 && kc <= 3) { pageX(kc + 3, (2 * kc + 6) & 7); if constexpr (ROLE == 0) pageW1(kc + 3, (2 * kc + 7) & 7); }
          if constexpr (kc == 4 && ROLE == 0) { pageW2(0, 6); pageW2(1, 7); }
          if constexpr (kc == 5) { pageLin(0, 0, 4); pageLin(16384, 1, 4); }
          if constexpr (kc == 6) { pageLin(32768, 2, 4); pageLin(49152, 3, 2); }
        } else if constexpr (ROLE == 0 && kc == 0) {
          asm volatile("" : "+v"(b1v[0]), "+v"(b1v[1]));
          if (w != 7) {
            f32x16 d = {0};
            d = mfma(pA0, pB0, d); d = mfma(pA1, pB1, d);
            const unsigned so = WS_WP + r * WP_HEAD + (g == 0 ? WP_Q + 2 * c * 1024 : 2 * c * 2048 + (g - 1) * 1024);
            vstore<0>(pack8(d, 0), l16, sWS, so);
            vstore<0>(pack8(d, 8), l16, sWS, so + (g == 0 ? 1024 : 2048));
          }
        }
      }
      const u32x4 xv = xf[4 * kc + tt];
      if constexpr (ROLE == 0) {
        acc[0] = mfma(af[tt][0], xv, acc[0]);
        acc[1] = mfma(af[tt][1], xv, acc[1]);
        if constexpr (tt == 0) {
#pragma unroll
          for (int i = 0; i < 4; i++) { cs[0] = dot2(af[0][0][i], ONES, cs[0]); cs[1] = dot2(af[0][1][i], ONES, cs[1]); }
        }
      }
      st0 = dot2(xv[0], CH == 0 ? ONES : xv[0], st0); st1 = dot2(xv[1], CH == 0 ? ONES : xv[1], st1);
      st0 = dot2(xv[2], CH == 0 ? ONES : xv[2], st0); st1 = dot2(xv[3], CH == 0 ? ONES : xv[3], st1);
    });
  });
  *(LDS3 float*)(lds + L_ST + CH * 1024 + (32 * g + lr) * 8 + hh * 4) = st0 + st1;
  if constexpr (ROLE == 0) {
    *(LDS3 float*)(lds + L_CS + (2 * CH) * 1024 + l16 + g * 4) = cs[0];
    *(LDS3 float*)(lds + L_CS + (2 * CH + 1) * 1024 + l16 + g * 4) = cs[1];
  }

  u32x4 mfrag;
  float rr, rl;
  f32x16 accB = {0};
  float csB = 0.f;
  const unsigned actv = (4 * r + g) * 144 * 1024 + l16;
  auto mish8 = [&](const f32x16& a, int o) {
    float t[8];
#pragma unroll
    for (int j = 0; j < 8; j++) {
      const float u = __builtin_amdgcn_exp2f(a[o + j] * rl);
      const float d = __builtin_fmaf(u, u + 2.f, 2.f);
      const float k = __builtin_fmaf(-2.f, __builtin_amdgcn_rcpf(d), 1.f);
      t[j] = (a[o + j] * rr) * k;
    }
    return u32x4{pk2(t[0], t[1]), pk2(t[2], t[3]), pk2(t[4], t[5]), pk2(t[6], t[7])};
  };
  sfor<0, 4>([&](auto V) {
    constexpr int v = V, k = 7 + v;
    bar<L::barN(k)>();
    TS(k);
    if constexpr (v == 0) {
      const f32x2 s1 = *(LDS3 f32x2*)(lds + L_ST + (32 * g + lr) * 8), s2 = *(LDS3 f32x2*)(lds + L_ST + 1024 + (32 * g + lr) * 8);
      const float m = (s1[0] + s1[1]) * (1.f / 448.f);
      const float var = fmaxf((s2[0] + s2[1]) * (1.f / 448.f) - m * m, 0.f) + 1e-5f;
      rr = __builtin_amdgcn_rsqf(var);
      const float sg = var * rr;
      rl = rr * 1.44269504088896f;
      const unsigned mh = pk2(-m, sg);
      const unsigned ml = pk2(-m - bflo(mh), sg - bfhi(mh));
      mfrag[0] = (mh & 0xffffu) | (ml << 16); mfrag[1] = mh; mfrag[2] = ml >> 16; mfrag[3] = 0;
      if constexpr (ROLE == 0) {
#pragma unroll
        for (int i = 0; i < 2; i++) {
          const float t = sum4(*(LDS3 f32x4*)(lds + L_CS + (2 * CH + i) * 1024 + l16));
          acc[i] = mfma(cfrag(t, hh ? 0u : b1v[i]), mfrag, acc[i]);
        }
      }
    }
    sfor<0, (v == 3 ? 4 : 8)>([&](auto S) {
      constexpr int s = S, kc = 2 * v + (s >> 2), tt = s & 3;
      const u32x4 f = ld128(lds + rot[tt] * 2048 + l16 + (CH * 1024 + v * SLOT + (kc & 1) * 8192));
      if constexpr (s == 0 && CH == 0) {
        if constexpr (v == 0) { pageLin(WP_Q, 4, 4); pageLin(WP_Q + 16384, 5, 3); }
        else if constexpr (ROLE == 0) pageW2(1 + v, v - 1);
      }
      if constexpr (CH == 0) accB = mfma(f, xf[4 * kc + tt], accB); else accB = mfma(xf[4 * kc + tt], f, accB);
      if constexpr (tt == 0) {
#pragma unroll
        for (int i = 0; i < 4; i++) csB = dot2(f[i], ONES, csB);
      }
    });
    if constexpr (ROLE == 0 && v < 2) {
      const unsigned so = WS_ACT + (8 * c + 4 * CH + 2 * v) * 1024;
      vstore<0>(mish8(acc[v], 0), actv, sWS, so);
      vstore<1024>(mish8(acc[v], 8), actv, sWS, so);
    }
  });
  *(LDS3 float*)(lds + L_CS + CH * 1024 + l16 + g * 4) = csB;

  bar<L::barN(11)>();
  TS(11);
  f32x16 accQ = {0};
  float csQ = 0.f;
  u32x4 phq[4];
  if constexpr (CH == 0) {
    const float t = sum4(*(LDS3 f32x4*)(lds + L_CS + l16));
    if constexpr (ROLE == 0) pageW2(5, 3);
    accB = mfma(cfrag(t, 0u), mfrag, accB);
    f32x16 p, n;
    dual_softmax(accB, rl, rr, hh != 0, p, n);
    const int T = 32 * g + lr;
    const unsigned gs = (T & 5) | ((T & 2) << 2) | ((T & 8) >> 2);
#pragma unroll
    for (int u = 0; u < 4; u++) {
      *(LDS3 u32x2*)(lds + L_PHI + T * 128 + (((2 * u + hh) ^ gs) << 3)) = u32x2{pk2(p[4 * u], p[4 * u + 1]), pk2(p[4 * u + 2], p[4 * u + 3])};
      *(LDS3 u32x2*)(lds + L_PHI + T * 128 + (((8 + 2 * u + hh) ^ gs) << 3)) = u32x2{pk2(n[4 * u], n[4 * u + 1]), pk2(n[4 * u + 2], n[4 * u + 3])};
    }
  } else {
    if constexpr (ROLE == 0) { if (w == 7) flag(ws, WS_AF + r * 128 + c * 8, tag, l); }
    const float t = sum4(*(LDS3 f32x4*)(lds + L_CS + 1024 + l16));
    accB = mfma(mfrag, cfrag(t, l == 28 ? 0x3F80u : 0u), accB);
    *(LDS3 u32x4*)(lds + L_VFR + 2 * g * 1024 + l16) = pack8(accB, 0);
    *(LDS3 u32x4*)(lds + L_VFR + (2 * g + 1) * 1024 + l16) = pack8(accB, 8);
    sfor<0, 16>([&](auto S) {
      constexpr int s = S, kc = s >> 2, tt = s & 3;
      const u32x4 f = ld128(lds + rot[tt] * 1024 + l16 + (4 * SLOT + kc * 4096));
      accQ = mfma(f, xf[4 * kc + tt], accQ);
      if constexpr (tt == 0) {
#pragma unroll
        for (int i = 0; i < 4; i++) csQ = dot2(f[i], ONES, csQ);
      }
    });
  }
  bar<L::barN(12)>();
  TS(12);
  if constexpr (CH == 0) {
    if constexpr (ROLE == 0) pageW2(6, 4);
  } else {
    if (g < 2) {
      f32x16 kv = {0};
      const int T0 = 4 * hh + q4;
      const unsigned gs0 = (T0 & 5) | ((T0 & 2) << 2);
      const unsigned a0 = L_PHI + T0 * 128 + (((8 * g + 4 * blk + p4) ^ gs0) << 3);
      const unsigned a1 = L_PHI + (T0 + 8) * 128 + (((8 * g + 4 * blk + p4) ^ gs0 ^ 2) << 3);
#pragma unroll
      for (int s = 0; s < 8; s++) kv = mfma(ldtr(lds + a0 + s * 2048, lds + a1 + s * 2048), ld128(lds + L_VFR + s * 1024 + l16), kv);
      const unsigned so = WS_KVP + ((c * 4 + 2 * g) * 16 + r) * 1024;
      vstore<0>(pack8(kv, 0), l16, sWS, so);
      vstore<0>(pack8(kv, 8), l16, sWS, so + 16384);
    }
    sfor<16, 28>([&](auto S) {
      constexpr int s = S, kc = s >> 2, tt = s & 3;
      const u32x4 f = ld128(lds + rot[tt] * 1024 + l16 + (5 * SLOT + (kc - 4) * 4096));
      accQ = mfma(f, xf[4 * kc + tt], accQ);
      if constexpr (tt == 0) {
#pragma unroll
        for (int i = 0; i < 4; i++) csQ = dot2(f[i], ONES, csQ);
      }
    });
    *(LDS3 float*)(lds + L_CS + 2048 + l16 + g * 4) = csQ;
    if constexpr (ROLE == 0) { if (w == 7) poll(ws, WS_AF + (R >> 1) * 128 + (l < 14 ? l : 0) * 8, tag); }
  }
  bar<L::barN(13)>();
  TS(13);
  u32x4 al[14][2];
  const unsigned actr = (2 * R + CH) * 144 * 1024 + 2 * g * 1024 + l16;
  if constexpr (ROLE == 0) {
    if constexpr (CH == 0) pageW2(7, 5);
    sfor<0, LA>([&](auto U) { constexpr int u = U; vload<true, 0>(al[u][0], actr, sWS, WS_ACT + u * 8192); vload<true, 1024>(al[u][1], actr, sWS, WS_ACT + u * 8192); });
  }
  if constexpr (CH == 1) {
    if (w == 7) flag(ws, WS_KF + c * 128 + r * 8, tag, l);
    const float t = sum4(*(LDS3 f32x4*)(lds + L_CS + 2048 + l16));
    accQ = mfma(cfrag(t, 0u), mfrag, accQ);
    f32x16 p, n;
    dual_softmax(accQ, rl, 1.f, hh != 0, p, n);
    phq[0] = pack8(p, 0); phq[1] = pack8(p, 8); phq[2] = pack8(n, 0); phq[3] = pack8(n, 8);
  }

  u32x4 t4[8];
  auto t4_load = [&]() {
    const unsigned so = WS_KVP + ((c * 4 + g) * 16 + 8 * CH) * 1024;
    vload<true, 0>(t4[0], l16, sWS, so); vload<true, 1024>(t4[1], l16, sWS, so); vload<true, 2048>(t4[2], l16, sWS, so); vload<true, 3072>(t4[3], l16, sWS, so);
    vload<true, 0>(t4[4], l16, sWS, so + 4096); vload<true, 1024>(t4[5], l16, sWS, so + 4096); vload<true, 2048>(t4[6], l16, sWS, so + 4096); vload<true, 3072>(t4[7], l16, sWS, so + 4096);
  };
  auto t4_sum = [&]() {
    f32x4 s0 = {0, 0, 0, 0}, s1 = {0, 0, 0, 0};
    unsigned KL = 0x00003F80u, KH = 0x3F800000u;
    asm volatile("" : "+s"(KL), "+s"(KH));
#pragma unroll
    for (int i = 0; i < 8; i++) {
      s0[0] = dot2(t4[i][0], KL, s0[0]); s0[1] = dot2(t4[i][0], KH, s0[1]);
      s0[2] = dot2(t4[i][1], KL, s0[2]); s0[3] = dot2(t4[i][1], KH, s0[3]);
      s1[0] = dot2(t4[i][2], KL, s1[0]); s1[1] = dot2(t4[i][2], KH, s1[1]);
      s1[2] = dot2(t4[i][3], KL, s1[2]); s1[3] = dot2(t4[i][3], KH, s1[3]);
    }
    *(LDS3 f32x4*)(lds + L_PHI + w * 2048 + l16) = s0;
    *(LDS3 f32x4*)(lds + L_PHI + w * 2048 + 1024 + l16) = s1;
  };
  auto t4_o = [&]() {
    f32x16 o = {0};
#pragma unroll
    for (int f = 0; f < 4; f++) {
      const f32x4 a0 = *(LDS3 f32x4*)(lds + L_PHI + f * 2048 + l16) + *(LDS3 f32x4*)(lds + L_PHI + (f + 4) * 2048 + l16);
      const f32x4 a1 = *(LDS3 f32x4*)(lds + L_PHI + f * 2048 + 1024 + l16) + *(LDS3 f32x4*)(lds + L_PHI + (f + 4) * 2048 + 1024 + l16);
      o = mfma(u32x4{pk2(a0[0], a0[1]), pk2(a0[2], a0[3]), pk2(a1[0], a1[1]), pk2(a1[2], a1[3])}, phq[f], o);
    }
    auto sw = __builtin_amdgcn_permlane32_swap(__float_as_uint(o[12]), __float_as_uint(o[12]), false, false);
    const float rc = __builtin_amdgcn_rcpf(__uint_as_float(sw[1]) + 1e-6f);
#pragma unroll
    for (int v = 0; v < 16; v++) o[v] = (v >= 12 && hh) ? 0.f : o[v] * rc;
    const unsigned so = WS_ACT + (112 + 2 * c) * 1024;
    vstore<0>(pack8(o, 0), actv, sWS, so);
    vstore<1024>(pack8(o, 8), actv, sWS, so);
  };

  if constexpr (ROLE == 1) {
    if constexpr (CH == 1) { if (w == 7) poll(ws, WS_KF + c * 128 + (l & 15) * 8, tag); }
    bar<63>();
    TS(14);
    t4_load();
    asm volatile("s_waitcnt vmcnt(0)" : "+v"(t4[0]), "+v"(t4[1]), "+v"(t4[2]), "+v"(t4[3]), "+v"(t4[4]), "+v"(t4[5]), "+v"(t4[6]), "+v"(t4[7]));
    t4_sum();
    bar<63>();
    TS(15);
    if constexpr (CH == 1) t4_o();
    bar<0>();
    TS(16);
    if constexpr (CH == 1) {
      if (w == 7) {
        flag(ws, WS_OF + r * 128 + c * 8, tag, l);
        if (r == 0 && c == 15) {
#pragma unroll
          for (int i = 0; i < 4; i++) poll(ws, WS_AF + i * 512 + ((l & 15) < 14 ? l : 0) * 8, tag);
          poll(ws, WS_KF + 14 * 128 + (l & 31) * 8, tag);
          flag(ws, 0, tag, l);
        }
      }
    }
  } else {
    f32x16 oa[2] = {{0}, {0}};
    unsigned ua[2];
#pragma unroll
    for (int i = 0; i < 2; i++) ua[i] = (4 * hh + q4) * 128 + ((((4 * i) ^ ((q4 >> 1) << 2)) + 2 * blk + (p4 >> 1)) << 4) + 8 * (p4 & 1);
    u32x4 of[8], xr;
    sfor<0, 14>([&](auto U) {
      constexpr int u = U, k = 14 + u;
      if constexpr (CH == 1 && k == KP) { if (w == 7) poll(ws, WS_KF + c * 128 + (l & 15) * 8, tag); }
      if constexpr (CH == 1 && k == OP) { if (w == 7) poll(ws, WS_OF + (R >> 1) * 128 + (l & 15) * 8, tag); }
      bar<L::barN(k)>();
      TS(k);
      if constexpr (CH == 1 && k == KP + 5) { if (w == 7) flag(ws, WS_OF + r * 128 + c * 8, tag, l); }
      constexpr unsigned su = ((u + 6) & 7) * SLOT;
      u32x4 a[2][2];
#pragma unroll
      for (int e = 0; e < 2; e++)
#pragma unroll
        for (int i = 0; i < 2; i++) a[e][i] = ldtr(lds + ua[i] + g * 4096 + (su + e * 2048), lds + ua[i] + g * 4096 + (su + e * 2048 + 1024));
      if constexpr (CH == 0) {
        if constexpr (u >= 1 && u <= 6) pageW2(7 + u, (u + 5) & 7);
        if constexpr (u >= 7 && u <= 10) pageWO(u - 7, u == 10 ? 2 : 4);
      }
      if constexpr (k == KP) t4_load();
      if constexpr (k == OP) {
        const unsigned so = WS_ACT + 112 * 1024;
        sfor<0, 4>([&](auto J) { constexpr int jj = J; vload<true, 0>(of[2 * jj], actr, sWS, so + jj * 8192); vload<true, 1024>(of[2 * jj + 1], actr, sWS, so + jj * 8192); });
        vload<false, 0>(xr, (64 * R + (threadIdx.x >> 3)) * 896 + (threadIdx.x & 7) * 16, sX, nt * 128);
      }
      if constexpr (u + LA <= 13) { vload<true, 0>(al[u + LA][0], actr, sWS, WS_ACT + (u + LA) * 8192); vload<true, 1024>(al[u + LA][1], actr, sWS, WS_ACT + (u + LA) * 8192); }
      asm volatile("s_waitcnt vmcnt(%2)" : "+v"(al[u][0]), "+v"(al[u][1]) : "n"(L::actN(u)));
#pragma unroll
      for (int e = 0; e < 2; e++)
#pragma unroll
        for (int i = 0; i < 2; i++) oa[i] = mfma(a[e][i], al[u][e], oa[i]);
      if constexpr (k == KP + 2) {
        asm volatile("s_waitcnt vmcnt(%8)" : "+v"(t4[0]), "+v"(t4[1]), "+v"(t4[2]), "+v"(t4[3]), "+v"(t4[4]), "+v"(t4[5]), "+v"(t4[6]), "+v"(t4[7]) : "n"(L::t4N()));
        t4_sum();
      }
      if constexpr (k == KP + 3 && CH == 1) t4_o();
    });
    bar<L::barN(28)>();
    TS(28);
    asm volatile("s_waitcnt vmcnt(%9)" : "+v"(of[0]), "+v"(of[1]), "+v"(of[2]), "+v"(of[3]), "+v"(of[4]), "+v"(of[5]), "+v"(of[6]), "+v"(of[7]), "+v"(xr) : "n"(L::oN()));
#pragma unroll
    for (int jj = 0; jj < 4; jj++)
#pragma unroll
      for (int t = 0; t < 2; t++)
#pragma unroll
        for (int i = 0; i < 2; i++) {
          LDS3 char* pb = lds + ua[i] + g * 3584 + (4 * SLOT + jj * 14336 + t * 2048);
          oa[i] = mfma(ldtr(pb, t == 0 ? pb + 1024 : pb + 1024 - hh * 512), of[2 * jj + t], oa[i]);
        }
    {
      const int T = 32 * CH + lr;
#pragma unroll
      for (int i = 0; i < 2; i++)
#pragma unroll
        for (int u = 0; u < 4; u++)
          *(LDS3 f32x4*)(lds + g * SLOT + T * 256 + (((8 * i + 2 * u + hh) ^ (T & 7)) << 4)) = f32x4{oa[i][4 * u], oa[i][4 * u + 1], oa[i][4 * u + 2], oa[i][4 * u + 3]};
    }
    bar<63>();
    TS(29);
    {
      const int T = threadIdx.x >> 3, ck = threadIdx.x & 7;
      f32x4 s0 = {0, 0, 0, 0}, s1 = {0, 0, 0, 0};
#pragma unroll
      for (int qq = 0; qq < 4; qq++) {
        s0 += *(LDS3 f32x4*)(lds + qq * SLOT + T * 256 + (((2 * ck) ^ (T & 7)) << 4));
        s1 += *(LDS3 f32x4*)(lds + qq * SLOT + T * 256 + (((2 * ck + 1) ^ (T & 7)) << 4));
      }
      u32x4 o;
      o[0] = pk2(s0[0] + bflo(xr[0]), s0[1] + bfhi(xr[0])); o[1] = pk2(s0[2] + bflo(xr[1]), s0[3] + bfhi(xr[1]));
      o[2] = pk2(s1[0] + bflo(xr[2]), s1[1] + bfhi(xr[2])); o[3] = pk2(s1[2] + bflo(xr[3]), s1[3] + bfhi(xr[3]));
      *(u32x4*)((char*)out + (64 * R + T) * 896 + nt * 128 + ck * 16) = o;
    }
  }
  TS(41); TR(43);
  if (blockIdx.x == 0 && w == 0) {
    const unsigned t0 = ttop;
    for (int it = 0; it < 1000000; it++) { if ((unsigned)__builtin_amdgcn_s_memrealtime() - t0 >= 10000u) break; __builtin_amdgcn_s_sleep(1); }
  }
}

__global__ __launch_bounds__(512, 2) void fused_kernel(const u16* __restrict__ x, const u16* __restrict__ wqkv, const u16* __restrict__ wphi,
                                                       const u16* __restrict__ wo, const u16* __restrict__ w1, const u16* __restrict__ b1,
                                                       const u16* __restrict__ w2, u16* __restrict__ out, char* ws) {
  unsigned long long tt_; asm volatile("s_memrealtime %0\n\ts_waitcnt lgkmcnt(0)" : "=s"(tt_)); const unsigned ttop = (unsigned)tt_;
  __shared__ __attribute__((aligned(16))) char lds_[LDS_BYTES];
  LDS3 char* lds = (LDS3 char*)lds_;
  const int w = __builtin_amdgcn_readfirstlane(threadIdx.x >> 6);
  const int b = blockIdx.x, xcd = b & 7, j = b >> 3, p = xcd >> 1;
  const int r = 4 * p + (j >> 3), c = 8 * (xcd & 1) + (j & 7);
  int R, nt;
  if ((xcd & 1) == 0) { R = 8 * p + (j >> 2); nt = j & 3; }
  else { const int m = (j >> 3) * 6 + (j & 7); R = 8 * p + m / 3; nt = 4 + m % 3; }
  if (c < 14) {
    if (w < 4) body<0, 0>(x, wqkv, wphi, wo, w1, b1, w2, out, ws, lds, w, r, c, R, nt, ttop);
    else body<0, 1>(x, wqkv, wphi, wo, w1, b1, w2, out, ws, lds, w, r, c, R, nt, ttop);
  } else {
    if (w < 4) body<1, 0>(x, wqkv, wphi, wo, w1, b1, w2, out, ws, lds, w, r, c, R, nt, ttop);
    else body<1, 1>(x, wqkv, wphi, wo, w1, b1, w2, out, ws, lds, w, r, c, R, nt, ttop);
  }
}

extern "C" void kernel_launch(void* const* d_in, const int* in_sizes, int n_in, void* d_out, int out_size, void* d_ws, size_t ws_size,
                              hipStream_t stream) {
  fused_kernel<<<256, 512, 0, stream>>>((const u16*)d_in[0], (const u16*)d_in[1], (const u16*)d_in[2], (const u16*)d_in[3], (const u16*)d_in[5],
                                        (const u16*)d_in[6], (const u16*)d_in[7], (u16*)d_out, (char*)d_ws);
}
```

```cpp
#include <hip/hip_runtime.h>
#include <stdint.h>
#include <type_traits>

typedef unsigned short u16;
typedef unsigned long long u64;
typedef __bf16 bf16x2 __attribute__((ext_vector_type(2)));
typedef __bf16 bf16x4 __attribute__((ext_vector_type(4)));
typedef __bf16 bf16x8 __attribute__((ext_vector_type(8)));
typedef float f32x2 __attribute__((ext_vector_type(2)));
typedef float f32x4 __attribute__((ext_vector_type(4)));
typedef float f32x16 __attribute__((ext_vector_type(16)));
typedef unsigned u32x2 __attribute__((ext_vector_type(2)));
typedef unsigned u32x4 __attribute__((ext_vector_type(4)));
#define LDS3 __attribute__((address_space(3)))
#define DEV __device__ __forceinline__

constexpr unsigned WS_PF = 1024, WS_AF = WS_PF + 2048, WS_KF = WS_AF + 2048, WS_OF = WS_KF + 2048;
constexpr unsigned WS_WP = 16384, WP_HEAD = 86016, WP_Q = 57344;
constexpr unsigned WS_KVP = WS_WP + 16 * WP_HEAD;
constexpr unsigned WS_ACT = WS_KVP + 16 * 4 * 16 * 1024;
constexpr unsigned WS_END = WS_ACT + 64 * 144 * 1024;
constexpr unsigned WS_DBG = WS_END;
constexpr unsigned SLOT = 16384;
constexpr unsigned L_PHI = 131072, L_VFR = L_PHI + 16384, L_ST = L_VFR + 8192, L_CS = L_ST + 2048;
constexpr unsigned LDS_BYTES = 163840;
#define LA 3
#define KP 16
#define OP 26
#define SPIN_MAX 2000000

template <int I, int N, class F> DEV void sfor(F&& f) {
  if constexpr (I < N) { f(std::integral_constant<int, I>{}); sfor<I + 1, N>(f); }
}
constexpr int cmin(int a, int b) { return a < b ? a : b; }
constexpr int cmax(int a, int b) { return a > b ? a : b; }

template <int ROLE, int CH> struct Led {
  static constexpr int pp = ROLE == 0 ? 8 : 4;
  static constexpr int nb1 = (ROLE == 0 && CH == 0) ? 2 : 0;
  static constexpr int pre = CH == 0 ? nb1 + 4 * pp : 0;
  static constexpr int G(int k) {
    if (CH != 0) return 0;
    if (k >= 1 && k <= 3) return pp;
    if (k == 5) return 8;
    if (k == 6) return 6;
    if (k == 7) return 7;
    if (ROLE != 0) return 0;
    if (k == 4) return 8;
    if (k >= 8 && k <= 13) return 4;
    if (k >= 15 && k <= 23) return 4;
    if (k == 24) return 2;
    return 0;
  }
  static constexpr int LX(int k) {
    if (ROLE != 0) return 0;
    return (k == KP ? 8 : 0) + (k == OP ? 9 : 0);
  }
  static constexpr int LAct(int k) {
    if (ROLE != 0) return 0;
    if (k == 13) return 2 * LA;
    if (k >= 14 && k - 14 + LA <= 13) return 2;
    return 0;
  }
  static constexpr int St(int k) {
    if (ROLE != 0) return 0;
    if (k == 7 || k == 8) return 2;
    if (CH == 1 && k == KP + 3) return 2;
    return 0;
  }
  static constexpr int cum(int k) { int s = pre; for (int i = 0; i < k; i++) s += G(i) + LX(i) + LAct(i) + St(i); return s; }
  static constexpr int need(int k) {
    if (CH == 0) {
      if (k <= 3) return nb1 + (k + 1) * pp;
      if (k <= 6) return cum(k - 3) + pp;
      if (k == 7) return cum(5) + 4;
      if (k == 8) return cum(5) + 8;
      if (k == 9) return cum(6) + 4;
      if (k == 10) return cum(6) + 6;
      if (k == 11) return ROLE == 0 ? cum(9) : cum(7) + 4;
      if (k == 12) return ROLE == 0 ? cum(9) : cum(7) + 7;
      if (ROLE != 0) return -1;
      if (k >= 16 && k <= 21) return cum(k - 8) + 4;
      if (k >= 22 && k <= 27) return cum(k - 7) + 4;
      if (k == 28) return cum(24) + 2;
      return -1;
    } else {
      if (k == 2 || k == 11 || k == 13) return cum(k);
      if (ROLE == 0 && k == KP + 5) return cum(KP + 4);
      return -1;
    }
  }
  static constexpr int barN(int k) { int n = need(k); return n < 0 ? 63 : cmin(63, cum(k) - n); }
  static constexpr int mid(int k) { return cum(k) + G(k) + LX(k) + LAct(k); }
  static constexpr int actEnd(int u) { return u < LA ? cum(13) + G(13) + LX(13) + 2 * (u + 1) : mid(14 + u - LA); }
  static constexpr int actN(int u) { return cmin(63, mid(14 + u) - actEnd(u)); }
  static constexpr int t4N() { return cmin(63, mid(KP + 2) - (cum(KP) + G(KP) + 8)); }
  static constexpr int oN() { return cmin(63, cum(28) - (cum(OP) + G(OP) + LX(OP))); }
};

DEV u32x4 mksrd(const void* p, unsigned bytes) {
  u64 a = (u64)p;
  u32x4 s; s[0] = (unsigned)a; s[1] = (unsigned)(a >> 32) & 0xffffu; s[2] = bytes; s[3] = 0x00020000u;
  return s;
}
template <bool SC1> DEV void glds(unsigned m0, unsigned voff, u32x4 srd, unsigned soff) {
  if constexpr (SC1)
    asm volatile("s_mov_b32 m0, %0\n\ts_nop 0\n\tbuffer_load_dwordx4 %1, %2, %3 offen sc1 lds" ::"s"(m0), "v"(voff), "s"(srd), "s"(soff) : "memory");
  else
    asm volatile("s_mov_b32 m0, %0\n\ts_nop 0\n\tbuffer_load_dwordx4 %1, %2, %3 offen lds" ::"s"(m0), "v"(voff), "s"(srd), "s"(soff) : "memory");
}
template <bool SC1, int OFF> DEV void vload(u32x4& d, unsigned voff, u32x4 srd, unsigned soff) {
  if constexpr (SC1)
    asm volatile("buffer_load_dwordx4 %0, %1, %2, %3 offen offset:%4 sc1" : "=v"(d) : "v"(voff), "s"(srd), "s"(soff), "n"(OFF) : "memory");
  else
    asm volatile("buffer_load_dwordx4 %0, %1, %2, %3 offen offset:%4" : "=v"(d) : "v"(voff), "s"(srd), "s"(soff), "n"(OFF) : "memory");
}
template <int OFF> DEV void vstore(u32x4 d, unsigned voff, u32x4 srd, unsigned soff) {
  asm volatile("buffer_store_dwordx4 %0, %1, %2, %3 offen offset:%4 sc1\n\ts_nop 1" ::"v"(d), "v"(voff), "s"(srd), "s"(soff), "n"(OFF) : "memory");
}
template <int N> DEV void bar() { asm volatile("s_waitcnt vmcnt(%0) lgkmcnt(0)\n\ts_barrier" ::"n"(N) : "memory"); }

DEV f32x16 mfma(u32x4 a, u32x4 b, f32x16 c) {
  return __builtin_amdgcn_mfma_f32_32x32x16_bf16(__builtin_bit_cast(bf16x8, a), __builtin_bit_cast(bf16x8, b), c, 0, 0, 0);
}
DEV float dot2(unsigned a, unsigned b, float c) {
  return __builtin_amdgcn_fdot2_f32_bf16(__builtin_bit_cast(bf16x2, a), __builtin_bit_cast(bf16x2, b), c, false);
}
DEV unsigned pk2(float lo, float hi) { f32x2 v = {lo, hi}; return __builtin_bit_cast(unsigned, __builtin_convertvector(v, bf16x2)); }
DEV float bflo(unsigned v) { return __uint_as_float(v << 16); }
DEV float bfhi(unsigned v) { return __uint_as_float(v & 0xffff0000u); }
DEV u32x4 ld128(LDS3 char* p) { return *(LDS3 u32x4*)p; }
DEV u32x4 ldtr(LDS3 char* p0, LDS3 char* p1) {
  bf16x4 a = __builtin_amdgcn_ds_read_tr16_b64_v4bf16((LDS3 bf16x4*)p0);
  bf16x4 b = __builtin_amdgcn_ds_read_tr16_b64_v4bf16((LDS3 bf16x4*)p1);
  return __builtin_bit_cast(u32x4, __builtin_shufflevector(a, b, 0, 1, 2, 3, 4, 5, 6, 7));
}
DEV u32x4 pack8(const f32x16& a, int o) {
  u32x4 r; r[0] = pk2(a[o], a[o + 1]); r[1] = pk2(a[o + 2], a[o + 3]); r[2] = pk2(a[o + 4], a[o + 5]); r[3] = pk2(a[o + 6], a[o + 7]);
  return r;
}
DEV float xhalf_max(float v) { auto s = __builtin_amdgcn_permlane32_swap(__float_as_uint(v), __float_as_uint(v), false, false); return fmaxf(__uint_as_float(s[0]), __uint_as_float(s[1])); }
DEV float xhalf_min(float v) { auto s = __builtin_amdgcn_permlane32_swap(__float_as_uint(v), __float_as_uint(v), false, false); return fminf(__uint_as_float(s[0]), __uint_as_float(s[1])); }
DEV float xhalf_add(float v) { auto s = __builtin_amdgcn_permlane32_swap(__float_as_uint(v), __float_as_uint(v), false, false); return __uint_as_float(s[0]) + __uint_as_float(s[1]); }
DEV u32x4 cfrag(float cs, unsigned e16) {
  unsigned hi = pk2(cs, cs);
  float lo = cs - bflo(hi);
  u32x4 r; r[0] = hi; r[1] = (pk2(lo, 0.f) & 0xffffu) | (e16 << 16); r[2] = e16; r[3] = 0;
  return r;
}
DEV float sum4(f32x4 v) { return (v[0] + v[1]) + (v[2] + v[3]); }

DEV void dual_softmax(const f32x16& a, float rl, float sc, bool hi_half, f32x16& p, f32x16& n) {
  float t[16];
#pragma unroll
  for (int v = 0; v < 16; v++) t[v] = (v >= 12 && hi_half) ? a[0] : a[v];
  float mx = t[0], mn = t[0];
#pragma unroll
  for (int v = 1; v < 16; v++) { mx = fmaxf(mx, t[v]); mn = fminf(mn, t[v]); }
  mx = xhalf_max(mx); mn = xhalf_min(mn);
  float c1 = -mx * rl, c2 = mn * rl, s1 = 0.f, s2 = 0.f;
#pragma unroll
  for (int v = 0; v < 16; v++) {
    float e1 = __builtin_amdgcn_exp2f(__builtin_fmaf(t[v], rl, c1));
    float e2 = __builtin_amdgcn_exp2f(__builtin_fmaf(-t[v], rl, c2));
    if (v >= 12) { e1 = hi_half ? 0.f : e1; e2 = hi_half ? 0.f : e2; }
    p[v] = e1; n[v] = e2; s1 += e1; s2 += e2;
  }
  s1 = sc * __builtin_amdgcn_rcpf(xhalf_add(s1));
  s2 = sc * __builtin_amdgcn_rcpf(xhalf_add(s2));
#pragma unroll
  for (int v = 0; v < 16; v++) { p[v] *= s1; n[v] *= s2; }
}

DEV void poll(const char* ws, unsigned off, u64 tag) {
  const u64* p = (const u64*)(ws + off);
#pragma unroll 1
  for (int it = 0; it < SPIN_MAX; it++) {
    const u64 v = __hip_atomic_load(p, __ATOMIC_RELAXED, __HIP_MEMORY_SCOPE_AGENT);
    if (__builtin_amdgcn_ballot_w64(v != tag) == 0) break;
    __builtin_amdgcn_s_sleep(1);
  }
}
DEV void flag(char* ws, unsigned off, u64 tag, int l) {
  if (l == 0) __hip_atomic_store((u64*)(ws + off), tag, __ATOMIC_RELAXED, __HIP_MEMORY_SCOPE_AGENT);
}

#define TS(k) do {} while (0)

template <int ROLE, int CH>
DEV void body(const u16* __restrict__ x, const u16* __restrict__ wqkv, const u16* __restrict__ wphi, const u16* __restrict__ wo,
              const u16* __restrict__ w1, const u16* __restrict__ b1, const u16* __restrict__ w2, u16* __restrict__ out, char* ws,
              LDS3 char* lds, const int w, const int r, const int c, const int R, const int nt) {
  using L = Led<ROLE, CH>;
  const int l = threadIdx.x & 63, lr = l & 31, hh = l >> 5;
  const int g = w & 3;
  const int q4 = (l & 15) >> 2, p4 = l & 3, blk = (l >> 4) & 1;
  const unsigned ldsb = (unsigned)(size_t)lds;
  TS(40);
  const u32x4 sX = mksrd(x, 2048 * 896), sW1 = mksrd(w1, 448 * 3584), sW2 = mksrd(w2, 1792 * 896), sWO = mksrd(wo, 448 * 896);
  const u32x4 sWS = mksrd(ws, WS_END);
  const unsigned m0w = ldsb + g * 1024;
  const unsigned vX = (8 * g + (l >> 3)) * 896 + (((l & 7) ^ (((g & 1) << 2) | (l >> 4))) << 4);
  const unsigned vW1 = (4 * g + (l >> 4)) * 3584 + (((l & 15) ^ ((l >> 4) << 2)) << 4);
  const unsigned vW2 = (8 * g + (l >> 3)) * 896 + (((l & 7) ^ (((l >> 4) & 1) << 2)) << 4);
  const unsigned vLin = g * 1024 + l * 16;
  const unsigned l16 = l * 16;

  auto pageX = [&](int kc, int slot) {
#pragma unroll
    for (int j = 0; j < 4; j++) glds<false>(m0w + slot * SLOT + j * 4096, vX, sX, (128 * r + 32 * j) * 896 + kc * 128);
  };
  auto pageW1 = [&](int kc, int slot) {
#pragma unroll
    for (int j = 0; j < 4; j++) glds<false>(m0w + slot * SLOT + j * 4096, vW1, sW1, (64 * kc + 16 * j) * 3584 + c * 256);
  };
  auto pageW2 = [&](int u, int slot) {
#pragma unroll
    for (int j = 0; j < 4; j++) glds<false>(m0w + slot * SLOT + j * 4096, vW2, sW2, (128 * u + 32 * j) * 896 + nt * 128);
  };
  auto pageWO = [&](int u, int n) {
#pragma unroll
    for (int j = 0; j < n; j++) glds<false>(m0w + (4 + u) * SLOT + j * 4096, vW2, sWO, (128 * u + 32 * j) * 896 + nt * 128);
  };
  auto pageLin = [&](unsigned off, int slot, int n) {
#pragma unroll
    for (int j = 0; j < n; j++) glds<true>(m0w + slot * SLOT + j * 4096, vLin, sWS, WS_WP + c * WP_HEAD + off + j * 4096);
  };

  unsigned b1v[2] = {0, 0};
  u64 tag = 0;
  u32x4 pA0 = {0, 0, 0, 0}, pA1 = {0, 0, 0, 0}, pB0 = {0, 0, 0, 0}, pB1 = {0, 0, 0, 0};
  if constexpr (CH == 0) {
    if constexpr (ROLE == 0) {
      const u32x4 sB1 = mksrd(b1, 1792 * 2);
      const unsigned vb = (128 * c + lr) * 2;
      asm volatile("buffer_load_ushort %0, %1, %2, 0 offen" : "=v"(b1v[0]) : "v"(vb), "s"(sB1) : "memory");
      asm volatile("buffer_load_ushort %0, %1, %2, 0 offen offset:64" : "=v"(b1v[1]) : "v"(vb), "s"(sB1) : "memory");
    }
#pragma unroll
    for (int kc = 0; kc < 4; kc++) { pageX(kc, 2 * kc); if constexpr (ROLE == 0) pageW1(kc, 2 * kc + 1); }
  } else {
    if (w == 7) tag = __hip_atomic_load((const u64*)ws, __ATOMIC_RELAXED, __HIP_MEMORY_SCOPE_AGENT) + 1;
    if constexpr (ROLE == 0) {
      b1v[0] = b1[128 * c + 64 + lr]; b1v[1] = b1[128 * c + 96 + lr];
      if (w != 7) {
        const int kk = (lr & 0x13) | ((lr & 4) << 1) | ((lr & 8) >> 1);
        const char* a = (const char*)wqkv + (32 * c + kk) * 2688 + (g * 448 + 28 * r + 8 * hh) * 2;
        u32x2 a0 = *(const u32x2*)a, a1 = *(const u32x2*)(a + 8), a2 = *(const u32x2*)(a + 32), a3 = *(const u32x2*)(a + (hh ? 32 : 40));
        pA0 = u32x4{a0[0], a0[1], a1[0], a1[1]}; pA1 = u32x4{a2[0], a2[1], a3[0], a3[1]};
        if (g < 2) {
          __amdgpu_buffer_rsrc_t rp = __builtin_amdgcn_make_buffer_rsrc((void*)wphi, 0, 16 * 784 * 2, 0x00020000);
          unsigned e[16];
#pragma unroll
          for (int i = 0; i < 16; i++) {
            const int d = (i >> 3) * 16 + 8 * hh + (i & 7);
            const unsigned o = (d < 28 && lr < 28) ? (unsigned)(r * 784 + d * 28 + lr) * 2u : 0x40000000u;
            e[i] = __builtin_amdgcn_raw_buffer_load_b16(rp, o, 0, 0);
          }
#pragma unroll
          for (int i = 0; i < 4; i++) { pB0[i] = e[2 * i] | (e[2 * i + 1] << 16); pB1[i] = e[8 + 2 * i] | (e[9 + 2 * i] << 16); }
        } else {
#pragma unroll
          for (int i = 0; i < 4; i++) {
            const int d = 8 * hh + 2 * i;
            pB0[i] = (lr == d ? 0x3F80u : 0u) | (lr == d + 1 ? 0x3F800000u : 0u);
            pB1[i] = lr < 28 ? ((lr == d + 16 ? 0x3F80u : 0u) | (lr == d + 17 ? 0x3F800000u : 0u)) : 0u;
          }
        }
      }
    }
  }

  unsigned xa[4], wa[4][2], rot[4];
#pragma unroll
  for (int tt = 0; tt < 4; tt++) {
    const int s = (tt + g) & 3, row = 32 * g + lr;
    xa[tt] = row * 128 + (((2 * s + hh) ^ ((row >> 1) & 7)) << 4);
#pragma unroll
    for (int i = 0; i < 2; i++) wa[tt][i] = (16 * s + 8 * hh + q4) * 256 + ((((8 * CH + 4 * i) ^ (q4 << 2)) + 2 * blk + (p4 >> 1)) << 4) + 8 * (p4 & 1);
    rot[tt] = s;
  }

  u32x4 xf[28];
  f32x16 acc[2] = {{0}, {0}};
  float cs[2] = {0.f, 0.f}, st0 = 0.f, st1 = 0.f;
  const unsigned ONES = 0x3F803F80u;

  sfor<0, 7>([&](auto KC) {
    constexpr int kc = KC;
    if constexpr (CH == 1 && kc == 5) { if (w == 7) poll(ws, WS_PF + c * 128 + (l < 14 ? l : 0) * 8, tag); }
    bar<L::barN(kc)>();
    TS(kc);
    if constexpr (CH == 0 && ROLE == 0 && kc == 0) asm volatile("" : "+v"(b1v[0]), "+v"(b1v[1]));
    if constexpr (CH == 1 && ROLE == 0 && kc == 2) { if (w == 7) flag(ws, WS_PF + r * 128 + c * 8, tag, l); }
    constexpr unsigned sx = ((2 * kc) & 7) * SLOT, sw = sx + SLOT;
    u32x4 af[4][2];
    sfor<0, 4>([&](auto TT) {
      constexpr int tt = TT;
      xf[4 * kc + tt] = ld128(lds + xa[tt] + sx);
      if constexpr (ROLE == 0) {
        af[tt][0] = ldtr(lds + wa[tt][0] + sw, lds + wa[tt][0] + sw + 1024);
        af[tt][1] = ldtr(lds + wa[tt][1] + sw, lds + wa[tt][1] + sw + 1024);
      }
      if constexpr (tt == 0) {
        if constexpr (CH == 0) {
          if constexpr (kc >= 1 && kc <= 3) { pageX(kc + 3, (2 * kc + 6) & 7); if constexpr (ROLE == 0) pageW1(kc + 3, (2 * kc + 7) & 7); }
          if constexpr (kc == 4 && ROLE == 0) { pageW2(0, 6); pageW2(1, 7); }
          if constexpr (kc == 5) { pageLin(0, 0, 4); pageLin(16384, 1, 4); }
          if constexpr (kc == 6) { pageLin(32768, 2, 4); pageLin(49152, 3, 2); }
        } else if constexpr (ROLE == 0 && kc == 0) {
          asm volatile("" : "+v"(b1v[0]), "+v"(b1v[1]));
          if (w != 7) {
            f32x16 d = {0};
            d = mfma(pA0, pB0, d); d = mfma(pA1, pB1, d);
            const unsigned so = WS_WP + r * WP_HEAD + (g == 0 ? WP_Q + 2 * c * 1024 : 2 * c * 2048 + (g - 1) * 1024);
            vstore<0>(pack8(d, 0), l16, sWS, so);
            vstore<0>(pack8(d, 8), l16, sWS, so + (g == 0 ? 1024 : 2048));
          }
        }
      }
      const u32x4 xv = xf[4 * kc + tt];
      if constexpr (ROLE == 0) {
        acc[0] = mfma(af[tt][0], xv, acc[0]);
        acc[1] = mfma(af[tt][1], xv, acc[1]);
        if constexpr (tt == 0) {
#pragma unroll
          for (int i = 0; i < 4; i++) { cs[0] = dot2(af[0][0][i], ONES, cs[0]); cs[1] = dot2(af[0][1][i], ONES, cs[1]); }
        }
      }
      st0 = dot2(xv[0], CH == 0 ? ONES : xv[0], st0); st1 = dot2(xv[1], CH == 0 ? ONES : xv[1], st1);
      st0 = dot2(xv[2], CH == 0 ? ONES : xv[2], st0); st1 = dot2(xv[3], CH == 0 ? ONES : xv[3], st1);
    });
  });
  *(LDS3 float*)(lds + L_ST + CH * 1024 + (32 * g + lr) * 8 + hh * 4) = st0 + st1;
  if constexpr (ROLE == 0) {
    *(LDS3 float*)(lds + L_CS + (2 * CH) * 1024 + l16 + g * 4) = cs[0];
    *(LDS3 float*)(lds + L_CS + (2 * CH + 1) * 1024 + l16 + g * 4) = cs[1];
  }

  u32x4 mfrag;
  float rr, rl;
  f32x16 accB = {0};
  float csB = 0.f;
  const unsigned actv = (4 * r + g) * 144 * 1024 + l16;
  auto mish8 = [&](const f32x16& a, int o) {
    float t[8];
#pragma unroll
    for (int j = 0; j < 8; j++) {
      const float u = __builtin_amdgcn_exp2f(a[o + j] * rl);
      const float d = __builtin_fmaf(u, u + 2.f, 2.f);
      const float k = __builtin_fmaf(-2.f, __builtin_amdgcn_rcpf(d), 1.f);
      t[j] = (a[o + j] * rr) * k;
    }
    return u32x4{pk2(t[0], t[1]), pk2(t[2], t[3]), pk2(t[4], t[5]), pk2(t[6], t[7])};
  };
  sfor<0, 4>([&](auto V) {
    constexpr int v = V, k = 7 + v;
    bar<L::barN(k)>();
    TS(k);
    if constexpr (v == 0) {
      const f32x2 s1 = *(LDS3 f32x2*)(lds + L_ST + (32 * g + lr) * 8), s2 = *(LDS3 f32x2*)(lds + L_ST + 1024 + (32 * g + lr) * 8);
      const float m = (s1[0] + s1[1]) * (1.f / 448.f);
      const float var = fmaxf((s2[0] + s2[1]) * (1.f / 448.f) - m * m, 0.f) + 1e-5f;
      rr = __builtin_amdgcn_rsqf(var);
      const float sg = var * rr;
      rl = rr * 1.44269504088896f;
      const unsigned mh = pk2(-m, sg);
      const unsigned ml = pk2(-m - bflo(mh), sg - bfhi(mh));
      mfrag[0] = (mh & 0xffffu) | (ml << 16); mfrag[1] = mh; mfrag[2] = ml >> 16; mfrag[3] = 0;
      if constexpr (ROLE == 0) {
#pragma unroll
        for (int i = 0; i < 2; i++) {
          const float t = sum4(*(LDS3 f32x4*)(lds + L_CS + (2 * CH + i) * 1024 + l16));
          acc[i] = mfma(cfrag(t, hh ? 0u : b1v[i]), mfrag, acc[i]);
        }
      }
    }
    sfor<0, (v == 3 ? 4 : 8)>([&](auto S) {
      constexpr int s = S, kc = 2 * v + (s >> 2), tt = s & 3;
      const u32x4 f = ld128(lds + rot[tt] * 2048 + l16 + (CH * 1024 + v * SLOT + (kc & 1) * 8192));
      if constexpr (s == 0 && CH == 0) {
        if constexpr (v == 0) { pageLin(WP_Q, 4, 4); pageLin(WP_Q + 16384, 5, 3); }
        else if constexpr (ROLE == 0) pageW2(1 + v, v - 1);
      }
      if constexpr (CH == 0) accB = mfma(f, xf[4 * kc + tt], accB); else accB = mfma(xf[4 * kc + tt], f, accB);
      if constexpr (tt == 0) {
#pragma unroll
        for (int i = 0; i < 4; i++) csB = dot2(f[i], ONES, csB);
      }
    });
    if constexpr (ROLE == 0 && v < 2) {
      const unsigned so = WS_ACT + (8 * c + 4 * CH + 2 * v) * 1024;
      vstore<0>(mish8(acc[v], 0), actv, sWS, so);
      vstore<1024>(mish8(acc[v], 8), actv, sWS, so);
    }
  });
  *(LDS3 float*)(lds + L_CS + CH * 1024 + l16 + g * 4) = csB;

  bar<L::barN(11)>();
  TS(11);
  f32x16 accQ = {0};
  float csQ = 0.f;
  u32x4 phq[4];
  if constexpr (CH == 0) {
    const float t = sum4(*(LDS3 f32x4*)(lds + L_CS + l16));
    if constexpr (ROLE == 0) pageW2(5, 3);
    accB = mfma(cfrag(t, 0u), mfrag, accB);
    f32x16 p, n;
    dual_softmax(accB, rl, rr, hh != 0, p, n);
    const int T = 32 * g + lr;
    const unsigned gs = (T & 5) | ((T & 2) << 2) | ((T & 8) >> 2);
#pragma unroll
    for (int u = 0; u < 4; u++) {
      *(LDS3 u32x2*)(lds + L_PHI + T * 128 + (((2 * u + hh) ^ gs) << 3)) = u32x2{pk2(p[4 * u], p[4 * u + 1]), pk2(p[4 * u + 2], p[4 * u + 3])};
      *(LDS3 u32x2*)(lds + L_PHI + T * 128 + (((8 + 2 * u + hh) ^ gs) << 3)) = u32x2{pk2(n[4 * u], n[4 * u + 1]), pk2(n[4 * u + 2], n[4 * u + 3])};
    }
  } else {
    if constexpr (ROLE == 0) { if (w == 7) flag(ws, WS_AF + r * 128 + c * 8, tag, l); }
    const float t = sum4(*(LDS3 f32x4*)(lds + L_CS + 1024 + l16));
    accB = mfma(mfrag, cfrag(t, l == 28 ? 0x3F80u : 0u), accB);
    *(LDS3 u32x4*)(lds + L_VFR + 2 * g * 1024 + l16) = pack8(accB, 0);
    *(LDS3 u32x4*)(lds + L_VFR + (2 * g + 1) * 1024 + l16) = pack8(accB, 8);
    sfor<0, 16>([&](auto S) {
      constexpr int s = S, kc = s >> 2, tt = s & 3;
      const u32x4 f = ld128(lds + rot[tt] * 1024 + l16 + (4 * SLOT + kc * 4096));
      accQ = mfma(f, xf[4 * kc + tt], accQ);
      if constexpr (tt == 0) {
#pragma unroll
        for (int i = 0; i < 4; i++) csQ = dot2(f[i], ONES, csQ);
      }
    });
  }
  bar<L::barN(12)>();
  TS(12);
  if constexpr (CH == 0) {
    if constexpr (ROLE == 0) pageW2(6, 4);
  } else {
    if (g < 2) {
      f32x16 kv = {0};
      const int T0 = 4 * hh + q4;
      const unsigned gs0 = (T0 & 5) | ((T0 & 2) << 2);
      const unsigned a0 = L_PHI + T0 * 128 + (((8 * g + 4 * blk + p4) ^ gs0) << 3);
      const unsigned a1 = L_PHI + (T0 + 8) * 128 + (((8 * g + 4 * blk + p4) ^ gs0 ^ 2) << 3);
#pragma unroll
      for (int s = 0; s < 8; s++) kv = mfma(ldtr(lds + a0 + s * 2048, lds + a1 + s * 2048), ld128(lds + L_VFR + s * 1024 + l16), kv);
      const unsigned so = WS_KVP + ((c * 4 + 2 * g) * 16 + r) * 1024;
      vstore<0>(pack8(kv, 0), l16, sWS, so);
      vstore<0>(pack8(kv, 8), l16, sWS, so + 16384);
    }
    sfor<16, 28>([&](auto S) {
      constexpr int s = S, kc = s >> 2, tt = s & 3;
      const u32x4 f = ld128(lds + rot[tt] * 1024 + l16 + (5 * SLOT + (kc - 4) * 4096));
      accQ = mfma(f, xf[4 * kc + tt], accQ);
      if constexpr (tt == 0) {
#pragma unroll
        for (int i = 0; i < 4; i++) csQ = dot2(f[i], ONES, csQ);
      }
    });
    *(LDS3 float*)(lds + L_CS + 2048 + l16 + g * 4) = csQ;
    if constexpr (ROLE == 0) { if (w == 7) poll(ws, WS_AF + (R >> 1) * 128 + (l < 14 ? l : 0) * 8, tag); }
  }
  bar<L::barN(13)>();
  TS(13);
  u32x4 al[14][2];
  const unsigned actr = (2 * R + CH) * 144 * 1024 + 2 * g * 1024 + l16;
  if constexpr (ROLE == 0) {
    if constexpr (CH == 0) pageW2(7, 5);
    sfor<0, LA>([&](auto U) { constexpr int u = U; vload<true, 0>(al[u][0], actr, sWS, WS_ACT + u * 8192); vload<true, 1024>(al[u][1], actr, sWS, WS_ACT + u * 8192); });
  }
  if constexpr (CH == 1) {
    if (w == 7) flag(ws, WS_KF + c * 128 + r * 8, tag, l);
    const float t = sum4(*(LDS3 f32x4*)(lds + L_CS + 2048 + l16));
    accQ = mfma(cfrag(t, 0u), mfrag, accQ);
    f32x16 p, n;
    dual_softmax(accQ, rl, 1.f, hh != 0, p, n);
    phq[0] = pack8(p, 0); phq[1] = pack8(p, 8); phq[2] = pack8(n, 0); phq[3] = pack8(n, 8);
  }

  u32x4 t4[8];
  auto t4_load = [&]() {
    const unsigned so = WS_KVP + ((c * 4 + g) * 16 + 8 * CH) * 1024;
    vload<true, 0>(t4[0], l16, sWS, so); vload<true, 1024>(t4[1], l16, sWS, so); vload<true, 2048>(t4[2], l16, sWS, so); vload<true, 3072>(t4[3], l16, sWS, so);
    vload<true, 0>(t4[4], l16, sWS, so + 4096); vload<true, 1024>(t4[5], l16, sWS, so + 4096); vload<true, 2048>(t4[6], l16, sWS, so + 4096); vload<true, 3072>(t4[7], l16, sWS, so + 4096);
  };
  auto t4_sum = [&]() {
    f32x4 s0 = {0, 0, 0, 0}, s1 = {0, 0, 0, 0};
    unsigned KL = 0x00003F80u, KH = 0x3F800000u;
    asm volatile("" : "+s"(KL), "+s"(KH));
#pragma unroll
    for (int i = 0; i < 8; i++) {
      s0[0] = dot2(t4[i][0], KL, s0[0]); s0[1] = dot2(t4[i][0], KH, s0[1]);
      s0[2] = dot2(t4[i][1], KL, s0[2]); s0[3] = dot2(t4[i][1], KH, s0[3]);
      s1[0] = dot2(t4[i][2], KL, s1[0]); s1[1] = dot2(t4[i][2], KH, s1[1]);
      s1[2] = dot2(t4[i][3], KL, s1[2]); s1[3] = dot2(t4[i][3], KH, s1[3]);
    }
    *(LDS3 f32x4*)(lds + L_PHI + w * 2048 + l16) = s0;
    *(LDS3 f32x4*)(lds + L_PHI + w * 2048 + 1024 + l16) = s1;
  };
  auto t4_o = [&]() {
    f32x16 o = {0};
#pragma unroll
    for (int f = 0; f < 4; f++) {
      const f32x4 a0 = *(LDS3 f32x4*)(lds + L_PHI + f * 2048 + l16) + *(LDS3 f32x4*)(lds + L_PHI + (f + 4) * 2048 + l16);
      const f32x4 a1 = *(LDS3 f32x4*)(lds + L_PHI + f * 2048 + 1024 + l16) + *(LDS3 f32x4*)(lds + L_PHI + (f + 4) * 2048 + 1024 + l16);
      o = mfma(u32x4{pk2(a0[0], a0[1]), pk2(a0[2], a0[3]), pk2(a1[0], a1[1]), pk2(a1[2], a1[3])}, phq[f], o);
    }
    auto sw = __builtin_amdgcn_permlane32_swap(__float_as_uint(o[12]), __float_as_uint(o[12]), false, false);
    const float rc = __builtin_amdgcn_rcpf(__uint_as_float(sw[1]) + 1e-6f);
#pragma unroll
    for (int v = 0; v < 16; v++) o[v] = (v >= 12 && hh) ? 0.f : o[v] * rc;
    const unsigned so = WS_ACT + (112 + 2 * c) * 1024;
    vstore<0>(pack8(o, 0), actv, sWS, so);
    vstore<1024>(pack8(o, 8), actv, sWS, so);
  };

  if constexpr (ROLE == 1) {
    if constexpr (CH == 1) { if (w == 7) poll(ws, WS_KF + c * 128 + (l & 15) * 8, tag); }
    bar<63>();
    TS(14);
    t4_load();
    asm volatile("s_waitcnt vmcnt(0)" : "+v"(t4[0]), "+v"(t4[1]), "+v"(t4[2]), "+v"(t4[3]), "+v"(t4[4]), "+v"(t4[5]), "+v"(t4[6]), "+v"(t4[7]));
    t4_sum();
    bar<63>();
    TS(15);
    if constexpr (CH == 1) t4_o();
    bar<0>();
    TS(16);
    if constexpr (CH == 1) {
      if (w == 7) {
        flag(ws, WS_OF + r * 128 + c * 8, tag, l);
        if (r == 0 && c == 15) {
#pragma unroll
          for (int i = 0; i < 4; i++) poll(ws, WS_AF + i * 512 + ((l & 15) < 14 ? l : 0) * 8, tag);
          poll(ws, WS_KF + 14 * 128 + (l & 31) * 8, tag);
          flag(ws, 0, tag, l);
        }
      }
    }
  } else {
    f32x16 oa[2] = {{0}, {0}};
    unsigned ua[2];
#pragma unroll
    for (int i = 0; i < 2; i++) ua[i] = (4 * hh + q4) * 128 + ((((4 * i) ^ ((q4 >> 1) << 2)) + 2 * blk + (p4 >> 1)) << 4) + 8 * (p4 & 1);
    u32x4 of[8], xr;
    sfor<0, 14>([&](auto U) {
      constexpr int u = U, k = 14 + u;
      if constexpr (CH == 1 && k == KP) { if (w == 7) poll(ws, WS_KF + c * 128 + (l & 15) * 8, tag); }
      if constexpr (CH == 1 && k == OP) { if (w == 7) poll(ws, WS_OF + (R >> 1) * 128 + (l & 15) * 8, tag); }
      bar<L::barN(k)>();
      TS(k);
      if constexpr (CH == 1 && k == KP + 5) { if (w == 7) flag(ws, WS_OF + r * 128 + c * 8, tag, l); }
      constexpr unsigned su = ((u + 6) & 7) * SLOT;
      u32x4 a[2][2];
#pragma unroll
      for (int e = 0; e < 2; e++)
#pragma unroll
        for (int i = 0; i < 2; i++) a[e][i] = ldtr(lds + ua[i] + g * 4096 + (su + e * 2048), lds + ua[i] + g * 4096 + (su + e * 2048 + 1024));
      if constexpr (CH == 0) {
        if constexpr (u >= 1 && u <= 6) pageW2(7 + u, (u + 5) & 7);
        if constexpr (u >= 7 && u <= 10) pageWO(u - 7, u == 10 ? 2 : 4);
      }
      if constexpr (k == KP) t4_load();
      if constexpr (k == OP) {
        const unsigned so = WS_ACT + 112 * 1024;
        sfor<0, 4>([&](auto J) { constexpr int jj = J; vload<true, 0>(of[2 * jj], actr, sWS, so + jj * 8192); vload<true, 1024>(of[2 * jj + 1], actr, sWS, so + jj * 8192); });
        vload<false, 0>(xr, (64 * R + (threadIdx.x >> 3)) * 896 + (threadIdx.x & 7) * 16, sX, nt * 128);
      }
      if constexpr (u + LA <= 13) { vload<true, 0>(al[u + LA][0], actr, sWS, WS_ACT + (u + LA) * 8192); vload<true, 1024>(al[u + LA][1], actr, sWS, WS_ACT + (u + LA) * 8192); }
      asm volatile("s_waitcnt vmcnt(%2)" : "+v"(al[u][0]), "+v"(al[u][1]) : "n"(L::actN(u)));
#pragma unroll
      for (int e = 0; e < 2; e++)
#pragma unroll
        for (int i = 0; i < 2; i++) oa[i] = mfma(a[e][i], al[u][e], oa[i]);
      if constexpr (k == KP + 2) {
        asm volatile("s_waitcnt vmcnt(%8)" : "+v"(t4[0]), "+v"(t4[1]), "+v"(t4[2]), "+v"(t4[3]), "+v"(t4[4]), "+v"(t4[5]), "+v"(t4[6]), "+v"(t4[7]) : "n"(L::t4N()));
        t4_sum();
      }
      if constexpr (k == KP + 3 && CH == 1) t4_o();
    });
    bar<L::barN(28)>();
    TS(28);
    asm volatile("s_waitcnt vmcnt(%9)" : "+v"(of[0]), "+v"(of[1]), "+v"(of[2]), "+v"(of[3]), "+v"(of[4]), "+v"(of[5]), "+v"(of[6]), "+v"(of[7]), "+v"(xr) : "n"(L::oN()));
#pragma unroll
    for (int jj = 0; jj < 4; jj++)
#pragma unroll
      for (int t = 0; t < 2; t++)
#pragma unroll
        for (int i = 0; i < 2; i++) {
          LDS3 char* pb = lds + ua[i] + g * 3584 + (4 * SLOT + jj * 14336 + t * 2048);
          oa[i] = mfma(ldtr(pb, t == 0 ? pb + 1024 : pb + 1024 - hh * 512), of[2 * jj + t], oa[i]);
        }
    {
      const int T = 32 * CH + lr;
#pragma unroll
      for (int i = 0; i < 2; i++)
#pragma unroll
        for (int u = 0; u < 4; u++)
          *(LDS3 f32x4*)(lds + g * SLOT + T * 256 + (((8 * i + 2 * u + hh) ^ (T & 7)) << 4)) = f32x4{oa[i][4 * u], oa[i][4 * u + 1], oa[i][4 * u + 2], oa[i][4 * u + 3]};
    }
    bar<63>();
    TS(29);
    {
      const int T = threadIdx.x >> 3, ck = threadIdx.x & 7;
      f32x4 s0 = {0, 0, 0, 0}, s1 = {0, 0, 0, 0};
#pragma unroll
      for (int qq = 0; qq < 4; qq++) {
        s0 += *(LDS3 f32x4*)(lds + qq * SLOT + T * 256 + (((2 * ck) ^ (T & 7)) << 4));
        s1 += *(LDS3 f32x4*)(lds + qq * SLOT + T * 256 + (((2 * ck + 1) ^ (T & 7)) << 4));
      }
      u32x4 o;
      o[0] = pk2(s0[0] + bflo(xr[0]), s0[1] + bfhi(xr[0])); o[1] = pk2(s0[2] + bflo(xr[1]), s0[3] + bfhi(xr[1]));
      o[2] = pk2(s1[0] + bflo(xr[2]), s1[1] + bfhi(xr[2])); o[3] = pk2(s1[2] + bflo(xr[3]), s1[3] + bfhi(xr[3]));
      { const u32x4 sO = mksrd(out, 2048 * 896); asm volatile("buffer_store_dwordx4 %0, %1, %2, %3 offen sc1\n\ts_nop 1" ::"v"(o), "v"((64 * R + T) * 896 + ck * 16), "s"(sO), "s"(nt * 128) : "memory"); }
    }
  }
}

__global__ __launch_bounds__(512, 2) void fused_kernel(const u16* __restrict__ x, const u16* __restrict__ wqkv, const u16* __restrict__ wphi,
                                                       const u16* __restrict__ wo, const u16* __restrict__ w1, const u16* __restrict__ b1,
                                                       const u16* __restrict__ w2, u16* __restrict__ out, char* ws) {
  __shared__ __attribute__((aligned(16))) char lds_[LDS_BYTES];
  LDS3 char* lds = (LDS3 char*)lds_;
  const int w = __builtin_amdgcn_readfirstlane(threadIdx.x >> 6);
  const int b = blockIdx.x, xcd = b & 7, j = b >> 3, p = xcd >> 1;
  const int r = 4 * p + (j >> 3), c = 8 * (xcd & 1) + (j & 7);
  int R, nt;
  if ((xcd & 1) == 0) { R = 8 * p + (j >> 2); nt = j & 3; }
  else { const int m = (j >> 3) * 6 + (j & 7); R = 8 * p + m / 3; nt = 4 + m % 3; }
  if (c < 14) {
    if (w < 4) body<0, 0>(x, wqkv, wphi, wo, w1, b1, w2, out, ws, lds, w, r, c, R, nt);
    else body<0, 1>(x, wqkv, wphi, wo, w1, b1, w2, out, ws, lds, w, r, c, R, nt);
  } else {
    if (w < 4) body<1, 0>(x, wqkv, wphi, wo, w1, b1, w2, out, ws, lds, w, r, c, R, nt);
    else body<1, 1>(x, wqkv, wphi, wo, w1, b1, w2, out, ws, lds, w, r, c, R, nt);
  }
}

extern "C" void kernel_launch(void* const* d_in, const int* in_sizes, int n_in, void* d_out, int out_size, void* d_ws, size_t ws_size,
                              hipStream_t stream) {
  fused_kernel<<<256, 512, 0, stream>>>((const u16*)d_in[0], (const u16*)d_in[1], (const u16*)d_in[2], (const u16*)d_in[3], (const u16*)d_in[5],
                                        (const u16*)d_in[6], (const u16*)d_in[7], (u16*)d_out, (char*)d_ws);
}
```

```cpp
#include <hip/hip_runtime.h>
#include <stdint.h>
#include <type_traits>
#define STAMP 1

typedef unsigned short u16;
typedef unsigned long long u64;
typedef __bf16 bf16x2 __attribute__((ext_vector_type(2)));
typedef __bf16 bf16x4 __attribute__((ext_vector_type(4)));
typedef __bf16 bf16x8 __attribute__((ext_vector_type(8)));
typedef float f32x2 __attribute__((ext_vector_type(2)));
typedef float f32x4 __attribute__((ext_vector_type(4)));
typedef float f32x16 __attribute__((ext_vector_type(16)));
typedef unsigned u32x2 __attribute__((ext_vector_type(2)));
typedef unsigned u32x4 __attribute__((ext_vector_type(4)));
#define LDS3 __attribute__((address_space(3)))
#define DEV __device__ __forceinline__

constexpr unsigned WS_PF = 1024, WS_AF = WS_PF + 2048, WS_KF = WS_AF + 2048, WS_OF = WS_KF + 2048;
constexpr unsigned WS_WP = 16384, WP_HEAD = 86016, WP_Q = 57344;
constexpr unsigned WS_KVP = WS_WP + 16 * WP_HEAD;
constexpr unsigned WS_ACT = WS_KVP + 16 * 4 * 16 * 1024;
constexpr unsigned WS_END = WS_ACT + 64 * 144 * 1024;
constexpr unsigned WS_DBG = WS_END;
constexpr unsigned SLOT = 16384;
constexpr unsigned L_PHI = 131072, L_VFR = L_PHI + 16384, L_ST = L_VFR + 8192, L_CS = L_ST + 2048;
constexpr unsigned LDS_BYTES = 163840;
#define LA 3
#define KP 16
#define OP 26
#define SPIN_MAX 2000000

template <int I, int N, class F> DEV void sfor(F&& f) {
  if constexpr (I < N) { f(std::integral_constant<int, I>{}); sfor<I + 1, N>(f); }
}
constexpr int cmin(int a, int b) { return a < b ? a : b; }
constexpr int cmax(int a, int b) { return a > b ? a : b; }

template <int ROLE, int CH> struct Led {
  static constexpr int pp = ROLE == 0 ? 8 : 4;
  static constexpr int nb1 = (ROLE == 0 && CH == 0) ? 2 : 0;
  static constexpr int pre = CH == 0 ? nb1 + 4 * pp : 0;
  static constexpr int G(int k) {
    if (CH != 0) return 0;
    if (k >= 1 && k <= 3) return pp;
    if (k == 5) return 8;
    if (k == 6) return 6;
    if (k == 7) return 7;
    if (ROLE != 0) return 0;
    if (k == 4) return 8;
    if (k >= 8 && k <= 13) return 4;
    if (k >= 15 && k <= 23) return 4;
    if (k == 24) return 2;
    return 0;
  }
  static constexpr int LX(int k) {
    if (ROLE != 0) return 0;
    return (k == KP ? 8 : 0) + (k == OP ? 9 : 0);
  }
  static constexpr int LAct(int k) {
    if (ROLE != 0) return 0;
    if (k == 13) return 2 * LA;
    if (k >= 14 && k - 14 + LA <= 13) return 2;
    return 0;
  }
  static constexpr int St(int k) {
    if (ROLE != 0) return 0;
    if (k == 7 || k == 8) return 2;
    if (CH == 1 && k == KP + 3) return 2;
    return 0;
  }
  static constexpr int cum(int k) { int s = pre; for (int i = 0; i < k; i++) s += G(i) + LX(i) + LAct(i) + St(i); return s; }
  static constexpr int need(int k) {
    if (CH == 0) {
      if (k <= 3) return nb1 + (k + 1) * pp;
      if (k <= 6) return cum(k - 3) + pp;
      if (k == 7) return cum(5) + 4;
      if (k == 8) return cum(5) + 8;
      if (k == 9) return cum(6) + 4;
      if (k == 10) return cum(6) + 6;
      if (k == 11) return ROLE == 0 ? cum(9) : cum(7) + 4;
      if (k == 12) return ROLE == 0 ? cum(9) : cum(7) + 7;
      if (ROLE != 0) return -1;
      if (k >= 16 && k <= 21) return cum(k - 8) + 4;
      if (k >= 22 && k <= 27) return cum(k - 7) + 4;
      if (k == 28) return cum(24) + 2;
      return -1;
    } else {
      if (k == 2 || k == 11 || k == 13) return cum(k);
      if (ROLE == 0 && k == KP + 5) return cum(KP + 4);
      return -1;
    }
  }
  static constexpr int barN(int k) { int n = need(k); return n < 0 ? 63 : cmin(63, cum(k) - n); }
  static constexpr int mid(int k) { return cum(k) + G(k) + LX(k) + LAct(k); }
  static constexpr int actEnd(int u) { return u < LA ? cum(13) + G(13) + LX(13) + 2 * (u + 1) : mid(14 + u - LA); }
  static constexpr int actN(int u) { return cmin(63, mid(14 + u) - actEnd(u)); }
  static constexpr int t4N() { return cmin(63, mid(KP + 2) - (cum(KP) + G(KP) + 8)); }
  static constexpr int oN() { return cmin(63, cum(28) - (cum(OP) + G(OP) + LX(OP))); }
};

DEV u32x4 mksrd(const void* p, unsigned bytes) {
  u64 a = (u64)p;
  u32x4 s; s[0] = (unsigned)a; s[1] = (unsigned)(a >> 32) & 0xffffu; s[2] = bytes; s[3] = 0x00020000u;
  return s;
}
template <bool SC1> DEV void glds(unsigned m0, unsigned voff, u32x4 srd, unsigned soff) {
  if constexpr (SC1)
    asm volatile("s_mov_b32 m0, %0\n\ts_nop 0\n\tbuffer_load_dwordx4 %1, %2, %3 offen sc1 lds" ::"s"(m0), "v"(voff), "s"(srd), "s"(soff) : "memory");
  else
    asm volatile("s_mov_b32 m0, %0\n\ts_nop 0\n\tbuffer_load_dwordx4 %1, %2, %3 offen lds" ::"s"(m0), "v"(voff), "s"(srd), "s"(soff) : "memory");
}
template <bool SC1, int OFF> DEV void vload(u32x4& d, unsigned voff, u32x4 srd, unsigned soff) {
  if constexpr (SC1)
    asm volatile("buffer_load_dwordx4 %0, %1, %2, %3 offen offset:%4 sc1" : "=v"(d) : "v"(voff), "s"(srd), "s"(soff), "n"(OFF) : "memory");
  else
    asm volatile("buffer_load_dwordx4 %0, %1, %2, %3 offen offset:%4" : "=v"(d) : "v"(voff), "s"(srd), "s"(soff), "n"(OFF) : "memory");
}
template <int OFF> DEV void vstore(u32x4 d, unsigned voff, u32x4 srd, unsigned soff) {
  asm volatile("buffer_store_dwordx4 %0, %1, %2, %3 offen offset:%4 sc1\n\ts_nop 1" ::"v"(d), "v"(voff), "s"(srd), "s"(soff), "n"(OFF) : "memory");
}
template <int N> DEV void bar() { asm volatile("s_waitcnt vmcnt(%0) lgkmcnt(0)\n\ts_barrier" ::"n"(N) : "memory"); }

DEV f32x16 mfma(u32x4 a, u32x4 b, f32x16 c) {
  return __builtin_amdgcn_mfma_f32_32x32x16_bf16(__builtin_bit_cast(bf16x8, a), __builtin_bit_cast(bf16x8, b), c, 0, 0, 0);
}
DEV float dot2(unsigned a, unsigned b, float c) {
  return __builtin_amdgcn_fdot2_f32_bf16(__builtin_bit_cast(bf16x2, a), __builtin_bit_cast(bf16x2, b), c, false);
}
DEV unsigned pk2(float lo, float hi) { f32x2 v = {lo, hi}; return __builtin_bit_cast(unsigned, __builtin_convertvector(v, bf16x2)); }
DEV float bflo(unsigned v) { return __uint_as_float(v << 16); }
DEV float bfhi(unsigned v) { return __uint_as_float(v & 0xffff0000u); }
DEV u32x4 ld128(LDS3 char* p) { return *(LDS3 u32x4*)p; }
DEV u32x4 ldtr(LDS3 char* p0, LDS3 char* p1) {
  bf16x4 a = __builtin_amdgcn_ds_read_tr16_b64_v4bf16((LDS3 bf16x4*)p0);
  bf16x4 b = __builtin_amdgcn_ds_read_tr16_b64_v4bf16((LDS3 bf16x4*)p1);
  return __builtin_bit_cast(u32x4, __builtin_shufflevector(a, b, 0, 1, 2, 3, 4, 5, 6, 7));
}
DEV u32x4 pack8(const f32x16& a, int o) {
  u32x4 r; r[0] = pk2(a[o], a[o + 1]); r[1] = pk2(a[o + 2], a[o + 3]); r[2] = pk2(a[o + 4], a[o + 5]); r[3] = pk2(a[o + 6], a[o + 7]);
  return r;
}
DEV float xhalf_max(float v) { auto s = __builtin_amdgcn_permlane32_swap(__float_as_uint(v), __float_as_uint(v), false, false); return fmaxf(__uint_as_float(s[0]), __uint_as_float(s[1])); }
DEV float xhalf_min(float v) { auto s = __builtin_amdgcn_permlane32_swap(__float_as_uint(v), __float_as_uint(v), false, false); return fminf(__uint_as_float(s[0]), __uint_as_float(s[1])); }
DEV float xhalf_add(float v) { auto s = __builtin_amdgcn_permlane32_swap(__float_as_uint(v), __float_as_uint(v), false, false); return __uint_as_float(s[0]) + __uint_as_float(s[1]); }
DEV u32x4 cfrag(float cs, unsigned e16) {
  unsigned hi = pk2(cs, cs);
  float lo = cs - bflo(hi);
  u32x4 r; r[0] = hi; r[1] = (pk2(lo, 0.f) & 0xffffu) | (e16 << 16); r[2] = e16; r[3] = 0;
  return r;
}
DEV float sum4(f32x4 v) { return (v[0] + v[1]) + (v[2] + v[3]); }

DEV void dual_softmax(const f32x16& a, float rl, float sc, bool hi_half, f32x16& p, f32x16& n) {
  float t[16];
#pragma unroll
  for (int v = 0; v < 16; v++) t[v] = (v >= 12 && hi_half) ? a[0] : a[v];
  float mx = t[0], mn = t[0];
#pragma unroll
  for (int v = 1; v < 16; v++) { mx = fmaxf(mx, t[v]); mn = fminf(mn, t[v]); }
  mx = xhalf_max(mx); mn = xhalf_min(mn);
  float c1 = -mx * rl, c2 = mn * rl, s1 = 0.f, s2 = 0.f;
#pragma unroll
  for (int v = 0; v < 16; v++) {
    float e1 = __builtin_amdgcn_exp2f(__builtin_fmaf(t[v], rl, c1));
    float e2 = __builtin_amdgcn_exp2f(__builtin_fmaf(-t[v], rl, c2));
    if (v >= 12) { e1 = hi_half ? 0.f : e1; e2 = hi_half ? 0.f : e2; }
    p[v] = e1; n[v] = e2; s1 += e1; s2 += e2;
  }
  s1 = sc * __builtin_amdgcn_rcpf(xhalf_add(s1));
  s2 = sc * __builtin_amdgcn_rcpf(xhalf_add(s2));
#pragma unroll
  for (int v = 0; v < 16; v++) { p[v] *= s1; n[v] *= s2; }
}

DEV void poll(const char* ws, unsigned off, u64 tag) {
  const u64* p = (const u64*)(ws + off);
#pragma unroll 1
  for (int it = 0; it < SPIN_MAX; it++) {
    const u64 v = __hip_atomic_load(p, __ATOMIC_RELAXED, __HIP_MEMORY_SCOPE_AGENT);
    if (__builtin_amdgcn_ballot_w64(v != tag) == 0) break;
    __builtin_amdgcn_s_sleep(1);
  }
}
DEV void flag(char* ws, unsigned off, u64 tag, int l) {
  if (l == 0) __hip_atomic_store((u64*)(ws + off), tag, __ATOMIC_RELAXED, __HIP_MEMORY_SCOPE_AGENT);
}

#define TS(k) do { unsigned long long t_ = __builtin_amdgcn_s_memtime(); asm volatile("v_writelane_b32 %0, %1, %2" : "+v"(stampv) : "s"((unsigned)t_), "n"(k)); } while (0)
#define TR(k) do { unsigned long long t_ = __builtin_amdgcn_s_memrealtime(); asm volatile("v_writelane_b32 %0, %1, %2" : "+v"(stampv) : "s"((unsigned)t_), "n"(k)); } while (0)

template <int ROLE, int CH>
DEV void body(const u16* __restrict__ x, const u16* __restrict__ wqkv, const u16* __restrict__ wphi, const u16* __restrict__ wo,
              const u16* __restrict__ w1, const u16* __restrict__ b1, const u16* __restrict__ w2, u16* __restrict__ out, char* ws,
              LDS3 char* lds, const int w, const int r, const int c, const int R, const int nt, const unsigned ttop) {
  using L = Led<ROLE, CH>;
  const int l = threadIdx.x & 63, lr = l & 31, hh = l >> 5;
  const int g = w & 3;
  const int q4 = (l & 15) >> 2, p4 = l & 3, blk = (l >> 4) & 1;
  const unsigned ldsb = (unsigned)(size_t)lds;
  unsigned stampv = 0;
  TS(40); TR(42);
  const u32x4 sX = mksrd(x, 2048 * 896), sW1 = mksrd(w1, 448 * 3584), sW2 = mksrd(w2, 1792 * 896), sWO = mksrd(wo, 448 * 896);
  const u32x4 sWS = mksrd(ws, WS_END);
  const unsigned m0w = ldsb + g * 1024;
  const unsigned vX = (8 * g + (l >> 3)) * 896 + (((l & 7) ^ (((g & 1) << 2) | (l >> 4))) << 4);
  const unsigned vW1 = (4 * g + (l >> 4)) * 3584 + (((l & 15) ^ ((l >> 4) << 2)) << 4);
  const unsigned vW2 = (8 * g + (l >> 3)) * 896 + (((l & 7) ^ (((l >> 4) & 1) << 2)) << 4);
  const unsigned vLin = g * 1024 + l * 16;
  const unsigned l16 = l * 16;

  auto pageX = [&](int kc, int slot) {
#pragma unroll
    for (int j = 0; j < 4; j++) glds<false>(m0w + slot * SLOT + j * 4096, vX, sX, (128 * r + 32 * j) * 896 + kc * 128);
  };
  auto pageW1 = [&](int kc, int slot) {
#pragma unroll
    for (int j = 0; j < 4; j++) glds<false>(m0w + slot * SLOT + j * 4096, vW1, sW1, (64 * kc + 16 * j) * 3584 + c * 256);
  };
  auto pageW2 = [&](int u, int slot) {
#pragma unroll
    for (int j = 0; j < 4; j++) glds<false>(m0w + slot * SLOT + j * 4096, vW2, sW2, (128 * u + 32 * j) * 896 + nt * 128);
  };
  auto pageWO = [&](int u, int n) {
#pragma unroll
    for (int j = 0; j < n; j++) glds<false>(m0w + (4 + u) * SLOT + j * 4096, vW2, sWO, (128 * u + 32 * j) * 896 + nt * 128);
  };
  auto pageLin = [&](unsigned off, int slot, int n) {
#pragma unroll
    for (int j = 0; j < n; j++) glds<true>(m0w + slot * SLOT + j * 4096, vLin, sWS, WS_WP + c * WP_HEAD + off + j * 4096);
  };

  unsigned b1v[2] = {0, 0};
  u64 tag = 0;
  u32x4 pA0 = {0, 0, 0, 0}, pA1 = {0, 0, 0, 0}, pB0 = {0, 0, 0, 0}, pB1 = {0, 0, 0, 0};
  if constexpr (CH == 0) {
    if constexpr (ROLE == 0) {
      const u32x4 sB1 = mksrd(b1, 1792 * 2);
      const unsigned vb = (128 * c + lr) * 2;
      asm volatile("buffer_load_ushort %0, %1, %2, 0 offen" : "=v"(b1v[0]) : "v"(vb), "s"(sB1) : "memory");
      asm volatile("buffer_load_ushort %0, %1, %2, 0 offen offset:64" : "=v"(b1v[1]) : "v"(vb), "s"(sB1) : "memory");
    }
#pragma unroll
    for (int kc = 0; kc < 4; kc++) { pageX(kc, 2 * kc); if constexpr (ROLE == 0) pageW1(kc, 2 * kc + 1); }
  } else {
    if (w == 7) tag = __hip_atomic_load((const u64*)ws, __ATOMIC_RELAXED, __HIP_MEMORY_SCOPE_AGENT) + 1;
    if constexpr (ROLE == 0) {
      b1v[0] = b1[128 * c + 64 + lr]; b1v[1] = b1[128 * c + 96 + lr];
      if (w != 7) {
        const int kk = (lr & 0x13) | ((lr & 4) << 1) | ((lr & 8) >> 1);
        const char* a = (const char*)wqkv + (32 * c + kk) * 2688 + (g * 448 + 28 * r + 8 * hh) * 2;
        u32x2 a0 = *(const u32x2*)a, a1 = *(const u32x2*)(a + 8), a2 = *(const u32x2*)(a + 32), a3 = *(const u32x2*)(a + (hh ? 32 : 40));
        pA0 = u32x4{a0[0], a0[1], a1[0], a1[1]}; pA1 = u32x4{a2[0], a2[1], a3[0], a3[1]};
        if (g < 2) {
          __amdgpu_buffer_rsrc_t rp = __builtin_amdgcn_make_buffer_rsrc((void*)wphi, 0, 16 * 784 * 2, 0x00020000);
          unsigned e[16];
#pragma unroll
          for (int i = 0; i < 16; i++) {
            const int d = (i >> 3) * 16 + 8 * hh + (i & 7);
            const unsigned o = (d < 28 && lr < 28) ? (unsigned)(r * 784 + d * 28 + lr) * 2u : 0x40000000u;
            e[i] = __builtin_amdgcn_raw_buffer_load_b16(rp, o, 0, 0);
          }
#pragma unroll
          for (int i = 0; i < 4; i++) { pB0[i] = e[2 * i] | (e[2 * i + 1] << 16); pB1[i] = e[8 + 2 * i] | (e[9 + 2 * i] << 16); }
        } else {
#pragma unroll
          for (int i = 0; i < 4; i++) {
            const int d = 8 * hh + 2 * i;
            pB0[i] = (lr == d ? 0x3F80u : 0u) | (lr == d + 1 ? 0x3F800000u : 0u);
            pB1[i] = lr < 28 ? ((lr == d + 16 ? 0x3F80u : 0u) | (lr == d + 17 ? 0x3F800000u : 0u)) : 0u;
          }
        }
      }
    }
  }

  unsigned xa[4], wa[4][2], rot[4];
#pragma unroll
  for (int tt = 0; tt < 4; tt++) {
    const int s = (tt + g) & 3, row = 32 * g + lr;
    xa[tt] = row * 128 + (((2 * s + hh) ^ ((row >> 1) & 7)) << 4);
#pragma unroll
    for (int i = 0; i < 2; i++) wa[tt][i] = (16 * s + 8 * hh + q4) * 256 + ((((8 * CH + 4 * i) ^ (q4 << 2)) + 2 * blk + (p4 >> 1)) << 4) + 8 * (p4 & 1);
    rot[tt] = s;
  }

  u32x4 xf[28];
  f32x16 acc[2] = {{0}, {0}};
  float cs[2] = {0.f, 0.f}, st0 = 0.f, st1 = 0.f;
  const unsigned ONES = 0x3F803F80u;

  sfor<0, 7>([&](auto KC) {
    constexpr int kc = KC;
    if constexpr (CH == 1 && kc == 5) { if (w == 7) poll(ws, WS_PF + c * 128 + (l < 14 ? l : 0) * 8, tag); }
    bar<L::barN(kc)>();
    TS(kc);
    if constexpr (CH == 0 && ROLE == 0 && kc == 0) asm volatile("" : "+v"(b1v[0]), "+v"(b1v[1]));
    if constexpr (CH == 1 && ROLE == 0 && kc == 2) { if (w == 7) flag(ws, WS_PF + r * 128 + c * 8, tag, l); }
    constexpr unsigned sx = ((2 * kc) & 7) * SLOT, sw = sx + SLOT;
    u32x4 af[4][2];
    sfor<0, 4>([&](auto TT) {
      constexpr int tt = TT;
      xf[4 * kc + tt] = ld128(lds + xa[tt] + sx);
      if constexpr (ROLE == 0) {
        af[tt][0] = ldtr(lds + wa[tt][0] + sw, lds + wa[tt][0] + sw + 1024);
        af[tt][1] = ldtr(lds + wa[tt][1] + sw, lds + wa[tt][1] + sw + 1024);
      }
      if constexpr (tt == 0) {
        if constexpr (CH == 0) {
          if constexpr (kc >= 1 && kc <= 3) { pageX(kc + 3, (2 * kc + 6) & 7); if constexpr (ROLE == 0) pageW1(kc + 3, (2 * kc + 7) & 7); }
          if constexpr (kc == 4 && ROLE == 0) { pageW2(0, 6); pageW2(1, 7); }
          if constexpr (kc == 5) { pageLin(0, 0, 4); pageLin(16384, 1, 4); }
          if constexpr (kc == 6) { pageLin(32768, 2, 4); pageLin(49152, 3, 2); }
        } else if constexpr (ROLE == 0 && kc == 0) {
          asm volatile("" : "+v"(b1v[0]), "+v"(b1v[1]));
          if (w != 7) {
            f32x16 d = {0};
            d = mfma(pA0, pB0, d); d = mfma(pA1, pB1, d);
            const unsigned so = WS_WP + r * WP_HEAD + (g == 0 ? WP_Q + 2 * c * 1024 : 2 * c * 2048 + (g - 1) * 1024);
            vstore<0>(pack8(d, 0), l16, sWS, so);
            vstore<0>(pack8(d, 8), l16, sWS, so + (g == 0 ? 1024 : 2048));
          }
        }
      }
      const u32x4 xv = xf[4 * kc + tt];
      if constexpr (ROLE == 0) {
        acc[0] = mfma(af[tt][0], xv, acc[0]);
        acc[1] = mfma(af[tt][1], xv, acc[1]);
        if constexpr (tt == 0) {
#pragma unroll
          for (int i = 0; i < 4; i++) { cs[0] = dot2(af[0][0][i], ONES, cs[0]); cs[1] = dot2(af[0][1][i], ONES, cs[1]); }
        }
      }
      st0 = dot2(xv[0], CH == 0 ? ONES : xv[0], st0); st1 = dot2(xv[1], CH == 0 ? ONES : xv[1], st1);
      st0 = dot2(xv[2], CH == 0 ? ONES : xv[2], st0); st1 = dot2(xv[3], CH == 0 ? ONES : xv[3], st1);
    });
  });
  *(LDS3 float*)(lds + L_ST + CH * 1024 + (32 * g + lr) * 8 + hh * 4) = st0 + st1;
  if constexpr (ROLE == 0) {
    *(LDS3 float*)(lds + L_CS + (2 * CH) * 1024 + l16 + g * 4) = cs[0];
    *(LDS3 float*)(lds + L_CS + (2 * CH + 1) * 1024 + l16 + g * 4) = cs[1];
  }

  u32x4 mfrag;
  float rr, rl;
  f32x16 accB = {0};
  float csB = 0.f;
  const unsigned actv = (4 * r + g) * 144 * 1024 + l16;
  auto mish8 = [&](const f32x16& a, int o) {
    float t[8];
#pragma unroll
    for (int j = 0; j < 8; j++) {
      const float u = __builtin_amdgcn_exp2f(a[o + j] * rl);
      const float d = __builtin_fmaf(u, u + 2.f, 2.f);
      const float k = __builtin_fmaf(-2.f, __builtin_amdgcn_rcpf(d), 1.f);
      t[j] = (a[o + j] * rr) * k;
    }
    return u32x4{pk2(t[0], t[1]), pk2(t[2], t[3]), pk2(t[4], t[5]), pk2(t[6], t[7])};
  };
  sfor<0, 4>([&](auto V) {
    constexpr int v = V, k = 7 + v;
    bar<L::barN(k)>();
    TS(k);
    if constexpr (v == 0) {
      const f32x2 s1 = *(LDS3 f32x2*)(lds + L_ST + (32 * g + lr) * 8), s2 = *(LDS3 f32x2*)(lds + L_ST + 1024 + (32 * g + lr) * 8);
      const float m = (s1[0] + s1[1]) * (1.f / 448.f);
      const float var = fmaxf((s2[0] + s2[1]) * (1.f / 448.f) - m * m, 0.f) + 1e-5f;
      rr = __builtin_amdgcn_rsqf(var);
      const float sg = var * rr;
      rl = rr * 1.44269504088896f;
      const unsigned mh = pk2(-m, sg);
      const unsigned ml = pk2(-m - bflo(mh), sg - bfhi(mh));
      mfrag[0] = (mh & 0xffffu) | (ml << 16); mfrag[1] = mh; mfrag[2] = ml >> 16; mfrag[3] = 0;
      if constexpr (ROLE == 0) {
#pragma unroll
        for (int i = 0; i < 2; i++) {
          const float t = sum4(*(LDS3 f32x4*)(lds + L_CS + (2 * CH + i) * 1024 + l16));
          acc[i] = mfma(cfrag(t, hh ? 0u : b1v[i]), mfrag, acc[i]);
        }
      }
    }
    sfor<0, (v == 3 ? 4 : 8)>([&](auto S) {
      constexpr int s = S, kc = 2 * v + (s >> 2), tt = s & 3;
      const u32x4 f = ld128(lds + rot[tt] * 2048 + l16 + (CH * 1024 + v * SLOT + (kc & 1) * 8192));
      if constexpr (s == 0 && CH == 0) {
        if constexpr (v == 0) { pageLin(WP_Q, 4, 4); pageLin(WP_Q + 16384, 5, 3); }
        else if constexpr (ROLE == 0) pageW2(1 + v, v - 1);
      }
      if constexpr (CH == 0) accB = mfma(f, xf[4 * kc + tt], accB); else accB = mfma(xf[4 * kc + tt], f, accB);
      if constexpr (tt == 0) {
#pragma unroll
        for (int i = 0; i < 4; i++) csB = dot2(f[i], ONES, csB);
      }
    });
    if constexpr (ROLE == 0 && v < 2) {
      const unsigned so = WS_ACT + (8 * c + 4 * CH + 2 * v) * 1024;
      vstore<0>(mish8(acc[v], 0), actv, sWS, so);
      vstore<1024>(mish8(acc[v], 8), actv, sWS, so);
    }
  });
  *(LDS3 float*)(lds + L_CS + CH * 1024 + l16 + g * 4) = csB;

  bar<L::barN(11)>();
  TS(11);
  f32x16 accQ = {0};
  float csQ = 0.f;
  u32x4 phq[4];
  if constexpr (CH == 0) {
    const float t = sum4(*(LDS3 f32x4*)(lds + L_CS + l16));
    if constexpr (ROLE == 0) pageW2(5, 3);
    accB = mfma(cfrag(t, 0u), mfrag, accB);
    f32x16 p, n;
    dual_softmax(accB, rl, rr, hh != 0, p, n);
    const int T = 32 * g + lr;
    const unsigned gs = (T & 5) | ((T & 2) << 2) | ((T & 8) >> 2);
#pragma unroll
    for (int u = 0; u < 4; u++) {
      *(LDS3 u32x2*)(lds + L_PHI + T * 128 + (((2 * u + hh) ^ gs) << 3)) = u32x2{pk2(p[4 * u], p[4 * u + 1]), pk2(p[4 * u + 2], p[4 * u + 3])};
      *(LDS3 u32x2*)(lds + L_PHI + T * 128 + (((8 + 2 * u + hh) ^ gs) << 3)) = u32x2{pk2(n[4 * u], n[4 * u + 1]), pk2(n[4 * u + 2], n[4 * u + 3])};
    }
  } else {
    if constexpr (ROLE == 0) { if (w == 7) flag(ws, WS_AF + r * 128 + c * 8, tag, l); }
    const float t = sum4(*(LDS3 f32x4*)(lds + L_CS + 1024 + l16));
    accB = mfma(mfrag, cfrag(t, l == 28 ? 0x3F80u : 0u), accB);
    *(LDS3 u32x4*)(lds + L_VFR + 2 * g * 1024 + l16) = pack8(accB, 0);
    *(LDS3 u32x4*)(lds + L_VFR + (2 * g + 1) * 1024 + l16) = pack8(accB, 8);
    sfor<0, 16>([&](auto S) {
      constexpr int s = S, kc = s >> 2, tt = s & 3;
      const u32x4 f = ld128(lds + rot[tt] * 1024 + l16 + (4 * SLOT + kc * 4096));
      accQ = mfma(f, xf[4 * kc + tt], accQ);
      if constexpr (tt == 0) {
#pragma unroll
        for (int i = 0; i < 4; i++) csQ = dot2(f[i], ONES, csQ);
      }
    });
  }
  bar<L::barN(12)>();
  TS(12);
  if constexpr (CH == 0) {
    if constexpr (ROLE == 0) pageW2(6, 4);
  } else {
    if (g < 2) {
      f32x16 kv = {0};
      const int T0 = 4 * hh + q4;
      const unsigned gs0 = (T0 & 5) | ((T0 & 2) << 2);
      const unsigned a0 = L_PHI + T0 * 128 + (((8 * g + 4 * blk + p4) ^ gs0) << 3);
      const unsigned a1 = L_PHI + (T0 + 8) * 128 + (((8 * g + 4 * blk + p4) ^ gs0 ^ 2) << 3);
#pragma unroll
      for (int s = 0; s < 8; s++) kv = mfma(ldtr(lds + a0 + s * 2048, lds + a1 + s * 2048), ld128(lds + L_VFR + s * 1024 + l16), kv);
      const unsigned so = WS_KVP + ((c * 4 + 2 * g) * 16 + r) * 1024;
      vstore<0>(pack8(kv, 0), l16, sWS, so);
      vstore<0>(pack8(kv, 8), l16, sWS, so + 16384);
    }
    sfor<16, 28>([&](auto S) {
      constexpr int s = S, kc = s >> 2, tt = s & 3;
      const u32x4 f = ld128(lds + rot[tt] * 1024 + l16 + (5 * SLOT + (kc - 4) * 4096));
      accQ = mfma(f, xf[4 * kc + tt], accQ);
      if constexpr (tt == 0) {
#pragma unroll
        for (int i = 0; i < 4; i++) csQ = dot2(f[i], ONES, csQ);
      }
    });
    *(LDS3 float*)(lds + L_CS + 2048 + l16 + g * 4) = csQ;
    if constexpr (ROLE == 0) { if (w == 7) poll(ws, WS_AF + (R >> 1) * 128 + (l < 14 ? l : 0) * 8, tag); }
  }
  bar<L::barN(13)>();
  TS(13);
  u32x4 al[14][2];
  const unsigned actr = (2 * R + CH) * 144 * 1024 + 2 * g * 1024 + l16;
  if constexpr (ROLE == 0) {
    if constexpr (CH == 0) pageW2(7, 5);
    sfor<0, LA>([&](auto U) { constexpr int u = U; vload<true, 0>(al[u][0], actr, sWS, WS_ACT + u * 8192); vload<true, 1024>(al[u][1], actr, sWS, WS_ACT + u * 8192); });
  }
  if constexpr (CH == 1) {
    if (w == 7) flag(ws, WS_KF + c * 128 + r * 8, tag, l);
    const float t = sum4(*(LDS3 f32x4*)(lds + L_CS + 2048 + l16));
    accQ = mfma(cfrag(t, 0u), mfrag, accQ);
    f32x16 p, n;
    dual_softmax(accQ, rl, 1.f, hh != 0, p, n);
    phq[0] = pack8(p, 0); phq[1] = pack8(p, 8); phq[2] = pack8(n, 0); phq[3] = pack8(n, 8);
  }

  u32x4 t4[8];
  auto t4_load = [&]() {
    const unsigned so = WS_KVP + ((c * 4 + g) * 16 + 8 * CH) * 1024;
    vload<true, 0>(t4[0], l16, sWS, so); vload<true, 1024>(t4[1], l16, sWS, so); vload<true, 2048>(t4[2], l16, sWS, so); vload<true, 3072>(t4[3], l16, sWS, so);
    vload<true, 0>(t4[4], l16, sWS, so + 4096); vload<true, 1024>(t4[5], l16, sWS, so + 4096); vload<true, 2048>(t4[6], l16, sWS, so + 4096); vload<true, 3072>(t4[7], l16, sWS, so + 4096);
  };
  auto t4_sum = [&]() {
    f32x4 s0 = {0, 0, 0, 0}, s1 = {0, 0, 0, 0};
    unsigned KL = 0x00003F80u, KH = 0x3F800000u;
    asm volatile("" : "+s"(KL), "+s"(KH));
#pragma unroll
    for (int i = 0; i < 8; i++) {
      s0[0] = dot2(t4[i][0], KL, s0[0]); s0[1] = dot2(t4[i][0], KH, s0[1]);
      s0[2] = dot2(t4[i][1], KL, s0[2]); s0[3] = dot2(t4[i][1], KH, s0[3]);
      s1[0] = dot2(t4[i][2], KL, s1[0]); s1[1] = dot2(t4[i][2], KH, s1[1]);
      s1[2] = dot2(t4[i][3], KL, s1[2]); s1[3] = dot2(t4[i][3], KH, s1[3]);
    }
    *(LDS3 f32x4*)(lds + L_PHI + w * 2048 + l16) = s0;
    *(LDS3 f32x4*)(lds + L_PHI + w * 2048 + 1024 + l16) = s1;
  };
  auto t4_o = [&]() {
    f32x16 o = {0};
#pragma unroll
    for (int f = 0; f < 4; f++) {
      const f32x4 a0 = *(LDS3 f32x4*)(lds + L_PHI + f * 2048 + l16) + *(LDS3 f32x4*)(lds + L_PHI + (f + 4) * 2048 + l16);
      const f32x4 a1 = *(LDS3 f32x4*)(lds + L_PHI + f * 2048 + 1024 + l16) + *(LDS3 f32x4*)(lds + L_PHI + (f + 4) * 2048 + 1024 + l16);
      o = mfma(u32x4{pk2(a0[0], a0[1]), pk2(a0[2], a0[3]), pk2(a1[0], a1[1]), pk2(a1[2], a1[3])}, phq[f], o);
    }
    auto sw = __builtin_amdgcn_permlane32_swap(__float_as_uint(o[12]), __float_as_uint(o[12]), false, false);
    const float rc = __builtin_amdgcn_rcpf(__uint_as_float(sw[1]) + 1e-6f);
#pragma unroll
    for (int v = 0; v < 16; v++) o[v] = (v >= 12 && hh) ? 0.f : o[v] * rc;
    const unsigned so = WS_ACT + (112 + 2 * c) * 1024;
    vstore<0>(pack8(o, 0), actv, sWS, so);
    vstore<1024>(pack8(o, 8), actv, sWS, so);
  };

  if constexpr (ROLE == 1) {
    if constexpr (CH == 1) { if (w == 7) poll(ws, WS_KF + c * 128 + (l & 15) * 8, tag); }
    bar<63>();
    TS(14);
    t4_load();
    asm volatile("s_waitcnt vmcnt(0)" : "+v"(t4[0]), "+v"(t4[1]), "+v"(t4[2]), "+v"(t4[3]), "+v"(t4[4]), "+v"(t4[5]), "+v"(t4[6]), "+v"(t4[7]));
    t4_sum();
    bar<63>();
    TS(15);
    if constexpr (CH == 1) t4_o();
    bar<0>();
    TS(16);
    if constexpr (CH == 1) {
      if (w == 7) {
        flag(ws, WS_OF + r * 128 + c * 8, tag, l);
        if (r == 0 && c == 15) {
#pragma unroll
          for (int i = 0; i < 4; i++) poll(ws, WS_AF + i * 512 + ((l & 15) < 14 ? l : 0) * 8, tag);
          poll(ws, WS_KF + 14 * 128 + (l & 31) * 8, tag);
          flag(ws, 0, tag, l);
        }
      }
    }
  } else {
    f32x16 oa[2] = {{0}, {0}};
    unsigned ua[2];
#pragma unroll
    for (int i = 0; i < 2; i++) ua[i] = (4 * hh + q4) * 128 + ((((4 * i) ^ ((q4 >> 1) << 2)) + 2 * blk + (p4 >> 1)) << 4) + 8 * (p4 & 1);
    u32x4 of[8], xr;
    sfor<0, 14>([&](auto U) {
      constexpr int u = U, k = 14 + u;
      if constexpr (CH == 1 && k == KP) { if (w == 7) poll(ws, WS_KF + c * 128 + (l & 15) * 8, tag); }
      if constexpr (CH == 1 && k == OP) { if (w == 7) poll(ws, WS_OF + (R >> 1) * 128 + (l & 15) * 8, tag); }
      bar<L::barN(k)>();
      TS(k);
      if constexpr (CH == 1 && k == KP + 5) { if (w == 7) flag(ws, WS_OF + r * 128 + c * 8, tag, l); }
      constexpr unsigned su = ((u + 6) & 7) * SLOT;
      u32x4 a[2][2];
#pragma unroll
      for (int e = 0; e < 2; e++)
#pragma unroll
        for (int i = 0; i < 2; i++) a[e][i] = ldtr(lds + ua[i] + g * 4096 + (su + e * 2048), lds + ua[i] + g * 4096 + (su + e * 2048 + 1024));
      if constexpr (CH == 0) {
        if constexpr (u >= 1 && u <= 6) pageW2(7 + u, (u + 5) & 7);
        if constexpr (u >= 7 && u <= 10) pageWO(u - 7, u == 10 ? 2 : 4);
      }
      if constexpr (k == KP) t4_load();
      if constexpr (k == OP) {
        const unsigned so = WS_ACT + 112 * 1024;
        sfor<0, 4>([&](auto J) { constexpr int jj = J; vload<true, 0>(of[2 * jj], actr, sWS, so + jj * 8192); vload<true, 1024>(of[2 * jj + 1], actr, sWS, so + jj * 8192); });
        vload<false, 0>(xr, (64 * R + (threadIdx.x >> 3)) * 896 + (threadIdx.x & 7) * 16, sX, nt * 128);
      }
      if constexpr (u + LA <= 13) { vload<true, 0>(al[u + LA][0], actr, sWS, WS_ACT + (u + LA) * 8192); vload<true, 1024>(al[u + LA][1], actr, sWS, WS_ACT + (u + LA) * 8192); }
      asm volatile("s_waitcnt vmcnt(%2)" : "+v"(al[u][0]), "+v"(al[u][1]) : "n"(L::actN(u)));
#pragma unroll
      for (int e = 0; e < 2; e++)
#pragma unroll
        for (int i = 0; i < 2; i++) oa[i] = mfma(a[e][i], al[u][e], oa[i]);
      if constexpr (k == KP + 2) {
        asm volatile("s_waitcnt vmcnt(%8)" : "+v"(t4[0]), "+v"(t4[1]), "+v"(t4[2]), "+v"(t4[3]), "+v"(t4[4]), "+v"(t4[5]), "+v"(t4[6]), "+v"(t4[7]) : "n"(L::t4N()));
        t4_sum();
      }
      if constexpr (k == KP + 3 && CH == 1) t4_o();
    });
    bar<L::barN(28)>();
    TS(28);
    asm volatile("s_waitcnt vmcnt(%9)" : "+v"(of[0]), "+v"(of[1]), "+v"(of[2]), "+v"(of[3]), "+v"(of[4]), "+v"(of[5]), "+v"(of[6]), "+v"(of[7]), "+v"(xr) : "n"(L::oN()));
#pragma unroll
    for (int jj = 0; jj < 4; jj++)
#pragma unroll
      for (int t = 0; t < 2; t++)
#pragma unroll
        for (int i = 0; i < 2; i++) {
          LDS3 char* pb = lds + ua[i] + g * 3584 + (4 * SLOT + jj * 14336 + t * 2048);
          oa[i] = mfma(ldtr(pb, t == 0 ? pb + 1024 : pb + 1024 - hh * 512), of[2 * jj + t], oa[i]);
        }
    {
      const int T = 32 * CH + lr;
#pragma unroll
      for (int i = 0; i < 2; i++)
#pragma unroll
        for (int u = 0; u < 4; u++)
          *(LDS3 f32x4*)(lds + g * SLOT + T * 256 + (((8 * i + 2 * u + hh) ^ (T & 7)) << 4)) = f32x4{oa[i][4 * u], oa[i][4 * u + 1], oa[i][4 * u + 2], oa[i][4 * u + 3]};
    }
    bar<63>();
    TS(29);
    {
      const int T = threadIdx.x >> 3, ck = threadIdx.x & 7;
      f32x4 s0 = {0, 0, 0, 0}, s1 = {0, 0, 0, 0};
#pragma unroll
      for (int qq = 0; qq < 4; qq++) {
        s0 += *(LDS3 f32x4*)(lds + qq * SLOT + T * 256 + (((2 * ck) ^ (T & 7)) << 4));
        s1 += *(LDS3 f32x4*)(lds + qq * SLOT + T * 256 + (((2 * ck + 1) ^ (T & 7)) << 4));
      }
      u32x4 o;
      o[0] = pk2(s0[0] + bflo(xr[0]), s0[1] + bfhi(xr[0])); o[1] = pk2(s0[2] + bflo(xr[1]), s0[3] + bfhi(xr[1]));
      o[2] = pk2(s1[0] + bflo(xr[2]), s1[1] + bfhi(xr[2])); o[3] = pk2(s1[2] + bflo(xr[3]), s1[3] + bfhi(xr[3]));
      *(u32x4*)((char*)out + (64 * R + T) * 896 + nt * 128 + ck * 16) = o;
    }
  }
  TS(41); TR(43);
  if (blockIdx.x == 16 && w == 0) *(volatile unsigned*)(ws + WS_DBG) = ttop;
}

__global__ __launch_bounds__(512, 2) void fused_kernel(const u16* __restrict__ x, const u16* __restrict__ wqkv, const u16* __restrict__ wphi,
                                                       const u16* __restrict__ wo, const u16* __restrict__ w1, const u16* __restrict__ b1,
                                                       const u16* __restrict__ w2, u16* __restrict__ out, char* ws) {
  unsigned long long tt_; asm volatile("s_memrealtime %0\n\ts_waitcnt lgkmcnt(0)" : "=s"(tt_)); const unsigned ttop = (unsigned)tt_;
  __shared__ __attribute__((aligned(16))) char lds_[LDS_BYTES];
  LDS3 char* lds = (LDS3 char*)lds_;
  const int w = __builtin_amdgcn_readfirstlane(threadIdx.x >> 6);
  const int b = blockIdx.x, xcd = b & 7, j = b >> 3, p = xcd >> 1;
  const int r = 4 * p + (j >> 3), c = 8 * (xcd & 1) + (j & 7);
  int R, nt;
  if ((xcd & 1) == 0) { R = 8 * p + (j >> 2); nt = j & 3; }
  else { const int m = (j >> 3) * 6 + (j & 7); R = 8 * p + m / 3; nt = 4 + m % 3; }
  if (c < 14) {
    if (w < 4) body<0, 0>(x, wqkv, wphi, wo, w1, b1, w2, out, ws, lds, w, r, c, R, nt, ttop);
    else body<0, 1>(x, wqkv, wphi, wo, w1, b1, w2, out, ws, lds, w, r, c, R, nt, ttop);
  } else {
    if (w < 4) body<1, 0>(x, wqkv, wphi, wo, w1, b1, w2, out, ws, lds, w, r, c, R, nt, ttop);
    else body<1, 1>(x, wqkv, wphi, wo, w1, b1, w2, out, ws, lds, w, r, c, R, nt, ttop);
  }
}

__global__ void k_tiny(char* ws) { const unsigned t0 = __builtin_amdgcn_readfirstlane(*(volatile unsigned*)(ws + WS_DBG));
    for (int it = 0; it < 1000000; it++) { if ((unsigned)__builtin_amdgcn_s_memrealtime() - t0 >= 10000u) break; __builtin_amdgcn_s_sleep(1); } }
extern "C" void kernel_launch(void* const* d_in, const int* in_sizes, int n_in, void* d_out, int out_size, void* d_ws, size_t ws_size,
                              hipStream_t stream) {
  fused_kernel<<<256, 512, 0, stream>>>((const u16*)d_in[0], (const u16*)d_in[1], (const u16*)d_in[2], (const u16*)d_in[3], (const u16*)d_in[5],
                                        (const u16*)d_in[6], (const u16*)d_in[7], (u16*)d_out, (char*)d_ws);
  k_tiny<<<1, 64, 0, stream>>>((char*)d_ws);
}
```

```cpp
#define PR 2
#define TW2 1
#include <hip/hip_runtime.h>
#include <stdint.h>
#include <type_traits>

typedef unsigned short u16;
typedef unsigned long long u64;
typedef __bf16 bf16x2 __attribute__((ext_vector_type(2)));
typedef __bf16 bf16x4 __attribute__((ext_vector_type(4)));
typedef __bf16 bf16x8 __attribute__((ext_vector_type(8)));
typedef float f32x2 __attribute__((ext_vector_type(2)));
typedef float f32x4 __attribute__((ext_vector_type(4)));
typedef float f32x16 __attribute__((ext_vector_type(16)));
typedef unsigned u32x2 __attribute__((ext_vector_type(2)));
typedef unsigned u32x4 __attribute__((ext_vector_type(4)));
#define LDS3 __attribute__((address_space(3)))
#define DEV __device__ __forceinline__
#define SB() __builtin_amdgcn_sched_barrier(0)

#define FS 128
constexpr unsigned WS_OF = 1024, WS_AF = WS_OF + 1024 * FS;
constexpr unsigned WS_ST = (WS_AF + 2048 * FS + 4095) / 4096 * 4096;
constexpr unsigned WS_KVP = WS_ST + 2048 * 16;
constexpr unsigned WS_KVR = WS_KVP + 64 * 16 * 2048;
constexpr unsigned WS_ACT = WS_KVR + 64 * 2048;
constexpr unsigned WS_OG = WS_ACT + 64 * 144 * 1024;
constexpr unsigned WS_END = WS_OG + 64 * 48 * 1024;
constexpr unsigned WS_DBG = WS_END;
constexpr unsigned OOB = 0x40000000u;
constexpr unsigned SLOT = 16384;
constexpr unsigned L_PHI = 131072, L_VFR = L_PHI + 16384, L_WF = L_VFR + 8192, L_CS = L_WF + 2048, L_PFD = L_CS + 4096;
constexpr unsigned LDS_BYTES = 163840;
#define LA 3
#define LA1 3
#define KP 3
#define OP 13
#define PFM 15
#define PFL 0
#define MAPT 1
#define WOFF 0
#define TS1 7
#define TA1 9
#define KE 2
#define XB 0
#define OP1 12
#define TL 1
#define OT 0
#define SF 0
#define EF 1
#define SPIN_MAX 2000000
enum { T_TAG = 0, T_X = 1, T_W = 10, T_KV01 = 30, T_KV23, T_Q, T_WM, T_AST, T_AF, T_ACT = 40, T_ST = 60, T_RF, T_KVR, T_OST, T_OF, T_XR, T_FL, T_N };
constexpr int VW[2][T_N] = {
{5,63,10,10,10,12,11,9,63,63,63,14,63,63,63,63,63,63,63,63,63,63,63,63,63,63,63,63,63,63,63,63,63,63,4,4,63,63,63,63,12,14,16,13,13,13,12,12,12,12,10,6,2,13,63,63,63,63,63,63,4,63,63,63,1,0,63},
{5,63,10,10,10,12,11,8,63,63,63,15,63,63,63,63,63,63,63,63,63,63,63,63,63,63,63,63,63,63,63,63,63,63,4,4,63,63,63,63,12,14,16,21,21,24,15,15,12,12,10,6,15,13,63,63,63,63,63,63,4,0,7,63,1,0,63}
};

template <int I, int N, class F> DEV void sfor(F&& f) {
  if constexpr (I < N) { f(std::integral_constant<int, I>{}); sfor<I + 1, N>(f); }
}

DEV u32x4 mksrd(const void* p, unsigned bytes) {
  u64 a = (u64)p;
  u32x4 s; s[0] = (unsigned)a; s[1] = (unsigned)(a >> 32) & 0xffffu; s[2] = bytes; s[3] = 0x00020000u;
  return s;
}
template <bool SC1, int TAG, unsigned KM, unsigned KS> DEV void glds(unsigned mb, unsigned voff, u32x4 srd, unsigned sb) {
  unsigned t;
  if constexpr (SC1)
    asm volatile("s_add_u32 m0, %1, %5\n\ts_add_u32 %0, %4, %6\n\tbuffer_load_dwordx4 %2, %3, %0 offen sc1 lds ; T%7"
                 : "=&s"(t) : "s"(mb), "v"(voff), "s"(srd), "s"(sb), "n"(KM), "n"(KS), "n"(TAG) : "memory", "scc");
  else
    asm volatile("s_add_u32 m0, %1, %5\n\ts_add_u32 %0, %4, %6\n\tbuffer_load_dwordx4 %2, %3, %0 offen lds ; T%7"
                 : "=&s"(t) : "s"(mb), "v"(voff), "s"(srd), "s"(sb), "n"(KM), "n"(KS), "n"(TAG) : "memory", "scc");
}
template <bool SC1, int TAG, int OFF> DEV void vload(u32x4& d, unsigned voff, u32x4 srd, unsigned soff) {
  if constexpr (SC1)
    asm volatile("buffer_load_dwordx4 %0, %1, %2, %3 offen offset:%4 sc1 ; T%5" : "=v"(d) : "v"(voff), "s"(srd), "s"(soff), "n"(OFF), "n"(TAG) : "memory");
  else
    asm volatile("buffer_load_dwordx4 %0, %1, %2, %3 offen offset:%4 ; T%5" : "=v"(d) : "v"(voff), "s"(srd), "s"(soff), "n"(OFF), "n"(TAG) : "memory");
}
template <int TAG> DEV void fload(u32x2& d, unsigned voff, u32x4 srd, unsigned soff) {
  asm volatile("buffer_load_dwordx2 %0, %1, %2, %3 offen sc1 ; T%4" : "=v"(d) : "v"(voff), "s"(srd), "s"(soff), "n"(TAG) : "memory");
}
template <int TAG, int OFF> DEV void vstore(u32x4 d, unsigned voff, u32x4 srd, unsigned soff) {
  asm volatile("buffer_store_dwordx4 %0, %1, %2, %3 offen offset:%4 sc1 ; T%5\n\ts_nop 1" ::"v"(d), "v"(voff), "s"(srd), "s"(soff), "n"(OFF), "n"(TAG) : "memory");
}
template <int TAG> DEV void ostore(u32x4 d, unsigned voff, u32x4 srd, unsigned soff) {
  if constexpr (SF == 0) vstore<TAG, 0>(d, voff, srd, soff);
  if constexpr (SF == 1) asm volatile("buffer_store_dwordx4 %0, %1, %2, %3 offen ; T%4\n\ts_nop 1" ::"v"(d), "v"(voff), "s"(srd), "s"(soff), "n"(TAG) : "memory");
  if constexpr (SF == 2) asm volatile("buffer_store_dwordx4 %0, %1, %2, %3 offen nt ; T%4\n\ts_nop 1" ::"v"(d), "v"(voff), "s"(srd), "s"(soff), "n"(TAG) : "memory");
  if constexpr (SF == 3) asm volatile("buffer_store_dwordx4 %0, %1, %2, %3 offen sc1 nt ; T%4\n\ts_nop 1" ::"v"(d), "v"(voff), "s"(srd), "s"(soff), "n"(TAG) : "memory");
}
template <int TAG> DEV void warm(unsigned mb, unsigned voff, u32x4 srd, unsigned sb) {
  asm volatile("s_mov_b32 m0, %0\n\ts_nop 0\n\tbuffer_load_dword %1, %2, %3 offen lds ; T%4" ::"s"(mb), "v"(voff), "s"(srd), "s"(sb), "n"(TAG) : "memory");
}
template <int TAG, int PM> DEV void prefetch(unsigned mb, int w, int l, const u16* x, const u16* wqkv, const u16* wo, const u16* w1, const u16* w2, const u16* out, const char* ws) {
  const int b = blockIdx.x, k = w >> 1, i = (w & 1) * 256 + b, ix = (w & 1) * 64 + (b >> 3) * 2 + (b & 1);
  const bool o = w == 1 && b >= 128;
  const u16* p = k == 0 ? (o ? wo : wqkv) : k == 1 ? w1 : k == 2 ? x : w2;
  unsigned n = k == 0 ? (o ? 448 * 896 : 448 * 2688) : k == 2 ? 2048 * 896 : 1792 * 896;
  unsigned c = o ? b - 128 : k == 2 ? (ix < 112 ? 112 * ((b & 7) >> 1) + ix : 511) : i;
  if (!((PM >> (o ? 3 : k)) & 1)) n = 0;
  warm<TAG>(mb, l * 64, mksrd(p, n), c * 4096);
}
DEV unsigned own_lines(int ch, int g, int l, int r, int c) {
  if (ch) return l < 40 ? WS_OG + ((4 * r + g) * 16 + c) * 3072 + l * 64 : l == 48 ? WS_AF + (((4 * r + g) * 2 + 1) * 16 + c) * FS : OOB;
  if (g < 2) return WS_KVP + ((c * 4 + 2 * g) * 16 + r) * 2048 + (l >> 5) * 32768 + (l & 31) * 64;
  return l < 2 ? WS_AF + ((4 * r + g - 2 * l) * 32 + c) * FS : OOB;
}
DEV void fstore(u32x2 d, unsigned voff, u32x4 srd, unsigned soff) {
  soff = __builtin_amdgcn_readfirstlane(soff);
  asm volatile("buffer_store_dwordx2 %0, %1, %2, %3 offen sc1 ; T%4\n\ts_nop 0" ::"v"(d), "v"(voff), "s"(srd), "s"(soff), "n"((int)T_FL) : "memory");
}
template <int N, int TAG> DEV void barw() { asm volatile("s_waitcnt vmcnt(%0) lgkmcnt(0) ; W%1\n\ts_barrier" ::"n"(N), "n"(TAG) : "memory"); }
template <int N> DEV void bar() { asm volatile("s_waitcnt vmcnt(%0) lgkmcnt(0)\n\ts_barrier" ::"n"(N) : "memory"); }
template <int N, int TAG> DEV void vwait() { asm volatile("s_waitcnt vmcnt(%0) ; W%1" ::"n"(N), "n"(TAG) : "memory"); }
template <int N, int TAG, class A> DEV void vwait(A& a) { asm volatile("s_waitcnt vmcnt(%1) ; W%2" : "+v"(a) : "n"(N), "n"(TAG) : "memory"); }
template <int N, int TAG, class A> DEV void vwait(A& a, A& b) { asm volatile("s_waitcnt vmcnt(%2) ; W%3" : "+v"(a), "+v"(b) : "n"(N), "n"(TAG) : "memory"); }
template <int N, int TAG, class A> DEV void vwait(A& a, A& b, A& c, A& d) {
  asm volatile("s_waitcnt vmcnt(%4) ; W%5" : "+v"(a), "+v"(b), "+v"(c), "+v"(d) : "n"(N), "n"(TAG) : "memory");
}
template <int TAG> DEV void need() { asm volatile("; N%0" ::"n"(TAG) : "memory"); }

DEV f32x16 mfma(u32x4 a, u32x4 b, f32x16 c) {
  return __builtin_amdgcn_mfma_f32_32x32x16_bf16(__builtin_bit_cast(bf16x8, a), __builtin_bit_cast(bf16x8, b), c, 0, 0, 0);
}
template <class A> DEV void pin(A& a) { asm volatile("" : "+v"(a)); }
template <class A, class... B> DEV void pin(A& a, B&... b) { pin(a); pin(b...); }
DEV void mf(f32x16& c, u32x4 a, u32x4 b) { c = mfma(a, b, c); pin(c); }
DEV void rowsum(f32x4& c, u32x4 sel, u32x4 f) {
  c = __builtin_amdgcn_mfma_f32_16x16x32_bf16(__builtin_bit_cast(bf16x8, sel), __builtin_bit_cast(bf16x8, f), c, 0, 0, 0);
  pin(c);
}
DEV u32x4 mksel(int l) { const unsigned v = (((l >> 4) ^ (l >> 2)) & 1) ? 0u : 0x3F003F00u; return u32x4{v, v, v, v}; }
DEV unsigned pk2(float lo, float hi) { f32x2 v = {lo, hi}; return __builtin_bit_cast(unsigned, __builtin_convertvector(v, bf16x2)); }
DEV float bflo(unsigned v) { return __uint_as_float(v << 16); }
DEV float bfhi(unsigned v) { return __uint_as_float(v & 0xffff0000u); }
DEV u32x4 ld128(LDS3 char* p) { return *(LDS3 u32x4*)p; }
DEV u32x2 ldtr1(LDS3 char* p) { return __builtin_bit_cast(u32x2, __builtin_amdgcn_ds_read_tr16_b64_v4bf16((LDS3 bf16x4*)p)); }
DEV u32x4 ldtr(LDS3 char* p0, LDS3 char* p1) {
  const u32x2 a = ldtr1(p0), b = ldtr1(p1);
  return u32x4{a[0], a[1], b[0], b[1]};
}
DEV u32x4 pack8(const f32x16& a, int o) {
  u32x4 r; r[0] = pk2(a[o], a[o + 1]); r[1] = pk2(a[o + 2], a[o + 3]); r[2] = pk2(a[o + 4], a[o + 5]); r[3] = pk2(a[o + 6], a[o + 7]);
  return r;
}
DEV float xhalf_max(float v) { auto s = __builtin_amdgcn_permlane32_swap(__float_as_uint(v), __float_as_uint(v), false, false); return fmaxf(__uint_as_float(s[0]), __uint_as_float(s[1])); }
DEV float xhalf_min(float v) { auto s = __builtin_amdgcn_permlane32_swap(__float_as_uint(v), __float_as_uint(v), false, false); return fminf(__uint_as_float(s[0]), __uint_as_float(s[1])); }
DEV float xhalf_add(float v) { auto s = __builtin_amdgcn_permlane32_swap(__float_as_uint(v), __float_as_uint(v), false, false); return __uint_as_float(s[0]) + __uint_as_float(s[1]); }
DEV float xrow_add(float v) { auto s = __builtin_amdgcn_permlane16_swap(__float_as_uint(v), __float_as_uint(v), false, false); return __uint_as_float(s[0]) + __uint_as_float(s[1]); }
DEV u32x4 cfrag(float cs, unsigned e16) {
  unsigned hi = pk2(cs, cs);
  float lo = cs - bflo(hi);
  u32x4 r; r[0] = hi; r[1] = (pk2(lo, 0.f) & 0xffffu) | (e16 << 16); r[2] = e16; r[3] = 0;
  return r;
}
DEV float sum4(f32x4 v) { float a = v[0] + v[1], b = v[2] + v[3]; pin(a, b); return a + b; }

template <int N> DEV void pinall(float (&v)[N]) {
#pragma unroll
  for (int i = 0; i < N; i++) pin(v[i]);
}
DEV void dual_softmax(const f32x16& a, float rl, float sc, bool hi_half, unsigned (&o)[16]) {
  float t[16], p[16], n[16];
#pragma unroll
  for (int v = 0; v < 16; v++) t[v] = (v >= 12 && hi_half) ? a[0] : a[v];
  float mx = fmaxf(t[0], t[1]), mn = fminf(t[0], t[1]), mx2 = fmaxf(t[2], t[3]), mn2 = fminf(t[2], t[3]);
#pragma unroll
  for (int v = 4; v < 16; v += 4) {
    mx = fmaxf(fmaxf(mx, t[v]), t[v + 1]); mn = fminf(fminf(mn, t[v]), t[v + 1]);
    mx2 = fmaxf(fmaxf(mx2, t[v + 2]), t[v + 3]); mn2 = fminf(fminf(mn2, t[v + 2]), t[v + 3]);
  }
  mx = xhalf_max(fmaxf(mx, mx2)); mn = xhalf_min(fminf(mn, mn2));
  const float c1 = -mx * rl, c2 = mn * rl;
  const float d1 = hi_half ? -__builtin_inff() : c1, d2 = hi_half ? -__builtin_inff() : c2;
#pragma unroll
  for (int v = 0; v < 16; v++) { p[v] = __builtin_fmaf(t[v], rl, v >= 12 ? d1 : c1); n[v] = __builtin_fmaf(-t[v], rl, v >= 12 ? d2 : c2); }
  pinall(p); pinall(n);
#pragma unroll
  for (int v = 0; v < 16; v++) { p[v] = __builtin_amdgcn_exp2f(p[v]); n[v] = __builtin_amdgcn_exp2f(n[v]); }
  float s[4] = {p[0], p[1], n[0], n[1]};
#pragma unroll
  for (int v = 2; v < 16; v += 2) { s[0] += p[v]; s[1] += p[v + 1]; s[2] += n[v]; s[3] += n[v + 1]; pinall(s); }
  const float s1 = sc * __builtin_amdgcn_rcpf(xhalf_add(s[0] + s[1]));
  const float s2 = sc * __builtin_amdgcn_rcpf(xhalf_add(s[2] + s[3]));
#pragma unroll
  for (int v = 0; v < 16; v++) { p[v] *= s1; n[v] *= s2; }
  pinall(p); pinall(n);
#pragma unroll
  for (int v = 0; v < 8; v++) { o[v] = pk2(p[2 * v], p[2 * v + 1]); o[8 + v] = pk2(n[2 * v], n[2 * v + 1]); }
}

DEV void check(u32x2 f, unsigned voff, u32x4 srd, unsigned soff, u64 tag, u64 mask) {
  unsigned n;
  soff = __builtin_amdgcn_readfirstlane(soff);
  asm volatile("s_mov_b32 %1, %6\n"
               "Lpoll%=:\n\tv_cmp_ne_u64 vcc, %5, %0\n\ts_and_b64 vcc, vcc, %7\n\ts_cbranch_vccz Ldone%=\n\t"
               "s_sleep 1\n\tbuffer_load_dwordx2 %0, %2, %3, %4 offen sc1\n\ts_sub_u32 %1, %1, 1\n\ts_waitcnt vmcnt(0)\n\ts_cbranch_scc0 Lpoll%=\n"
               "Ldone%=:"
               : "+v"(f), "=&s"(n) : "v"(voff), "s"(srd), "s"(soff), "s"(tag), "n"(SPIN_MAX), "s"(mask) : "memory", "vcc", "scc");
}
DEV void poll(unsigned voff, u32x4 srd, unsigned soff, u64 tag, u64 mask) { check(u32x2{~(unsigned)tag, 0u}, voff, srd, soff, tag, mask); }
DEV u64 mktag(u32x2 v) {
  return (((u64)__builtin_amdgcn_readfirstlane(v[1]) << 32) | (unsigned)__builtin_amdgcn_readfirstlane(v[0])) + 1;
}

template <int C> DEV float dpp_add(float v) { return v + __builtin_bit_cast(float, __builtin_amdgcn_update_dpp(0, __builtin_bit_cast(int, v), C, 0xF, 0xF, true)); }
DEV float wave_sum(float v) {
  v = dpp_add<0x128>(v); v = dpp_add<0x124>(v); v = dpp_add<0x122>(v); v = dpp_add<0x121>(v);
  return xhalf_add(xrow_add(v));
}
DEV u32x4 row_stats(u32x4 xv, int l, unsigned tg) {
  float e[8];
#pragma unroll
  for (int i = 0; i < 4; i++) { e[2 * i] = bflo(xv[i]); e[2 * i + 1] = bfhi(xv[i]); }
  pinall(e);
  float s0 = (e[0] + e[1]), s1 = (e[2] + e[3]), s2 = e[4] + e[5], s3 = e[6] + e[7];
  pin(s0, s1, s2, s3);
  const float nm = wave_sum((s0 + s1) + (s2 + s3)) * (-1.f / 448.f);
  const float nl = l < 56 ? nm : 0.f;
#pragma unroll
  for (int i = 0; i < 8; i++) e[i] += nl;
  pinall(e);
  float q0 = e[0] * e[0], q1 = e[1] * e[1];
#pragma unroll
  for (int i = 2; i < 8; i += 2) { q0 = __builtin_fmaf(e[i], e[i], q0); q1 = __builtin_fmaf(e[i + 1], e[i + 1], q1); pin(q0, q1); }
  const float var = __builtin_fmaf(wave_sum(q0 + q1), 1.f / 448.f, 1e-5f);
  return u32x4{__float_as_uint(nm), tg, __float_as_uint(var), tg};
}
struct Stat { u32x4 mfrag; float rr, rl; };
DEV Stat stats(u32x4 gr) {
  const float nm = __uint_as_float(gr[0]), var = __uint_as_float(gr[2]);
  Stat s;
  s.rr = __builtin_amdgcn_rsqf(var);
  float sg = var * s.rr;
  s.rl = s.rr * 1.44269504088896f;
  const unsigned mh = pk2(nm, sg);
  float l0 = nm - bflo(mh), l1 = sg - bfhi(mh);
  pin(l0, l1);
  const unsigned ml = pk2(l0, l1);
  s.mfrag[0] = (mh & 0xffffu) | (ml << 16); s.mfrag[1] = mh; s.mfrag[2] = ml >> 16; s.mfrag[3] = 0;
  return s;
}
template <int N> DEV bool stale(const u32x4 (&v)[N], unsigned tg) {
  unsigned b0 = 0, b1 = 0;
#pragma unroll
  for (int i = 0; i < N; i++) { b0 |= v[i][1] ^ tg; b1 |= v[i][3] ^ tg; }
  return __builtin_amdgcn_ballot_w64((b0 | b1) != 0) != 0;
}
template <int N, int STR> DEV void refetch(u32x4 (&v)[N], unsigned voff, u32x4 srd, unsigned soff, unsigned tg) {
  int it = SPIN_MAX;
  soff = __builtin_amdgcn_readfirstlane(soff);
  do {
#pragma unroll
    for (int i = 0; i < N; i++) vload<true, 99, 0>(v[i], voff, srd, soff + i * STR);
    asm volatile("s_waitcnt vmcnt(0)" ::: "memory");
#pragma unroll
    for (int i = 0; i < N; i++) pin(v[i]);
  } while (stale(v, tg) && --it > 0);
}
template <int TG> DEV void o_store(u32x4 o0, u32x4 o1, unsigned vo, u32x4 sWS, unsigned so, unsigned tg) {
  so = __builtin_amdgcn_readfirstlane(so);
  vstore<TG, 0>(u32x4{o0[0], o0[1], o0[2], tg}, vo, sWS, so); vstore<TG, 1024>(u32x4{o0[3], o1[0], o1[1], tg}, vo, sWS, so);
  vstore<TG, 2048>(u32x4{o1[2], o1[3], tg, tg}, vo < 512 ? vo : OOB, sWS, so);
}
template <int TG> DEV void o_load(u32x4 (&v)[12], unsigned vo, u32x4 sWS, unsigned so) {
  const unsigned vh = vo < 512 ? vo : OOB;
  sfor<0, 12>([&](auto I) { constexpr int i = I; vload<true, TG, (i % 3) * 1024>(v[i], i % 3 == 2 ? vh : vo, sWS, so + (i / 6) * 49152 + ((i / 3) & 1) * 3072); });
}
DEV bool o_stale(const u32x4 (&v)[12], unsigned tg) {
  unsigned b0 = 0, b1 = 0, b2 = 0;
#pragma unroll
  for (int i = 0; i < 4; i++) { b0 |= v[3 * i][3] ^ tg; b1 |= v[3 * i + 1][3] ^ tg; b2 |= v[3 * i + 2][3] ^ tg; }
  return (__builtin_amdgcn_ballot_w64((b0 | b1) != 0) | (__builtin_amdgcn_ballot_w64(b2 != 0) & 0xffffffffull)) != 0;
}
DEV u32x4 gran(unsigned a, unsigned b, unsigned tg) { return u32x4{a, tg, b, tg}; }
DEV f32x16 logits(LDS3 char* lds, f32x16 a, float cs, const Stat& st, int l) {
  const int lr = l & 31, hh = l >> 5;
  LDS3 char* pb = lds + L_WF + (4 * hh + ((l & 15) >> 2)) * 56 + ((l >> 4) & 1) * 32 + (l & 3) * 8;
  u32x4 f0 = ldtr(pb, pb + 8 * 56), f1 = ldtr(pb + 16 * 56, pb + 24 * 56);
  mf(a, cfrag(cs, 0u), st.mfrag);
#pragma unroll
  for (int i = 0; i < 4; i++) { f0[i] = lr < 28 ? f0[i] : 0u; f1[i] = (lr < 28 && !(hh && i >= 2)) ? f1[i] : 0u; }
  const u32x4 b0 = pack8(a, 0);
  u32x4 b1 = pack8(a, 8);
  b1[2] = hh ? 0u : b1[2]; b1[3] = hh ? 0u : b1[3];
  f32x16 d = {0};
  mf(d, f0, b0); mf(d, f1, b1);
  return d;
}
DEV void k_phase(LDS3 char* lds, f32x16 accB, float t, const Stat& st, int g, int l) {
  const int lr = l & 31, hh = l >> 5;
  accB = logits(lds, accB, t, st, l);
  unsigned o[16];
  dual_softmax(accB, st.rl, st.rr, hh != 0, o);
  const int T = 32 * g + lr;
  const unsigned gs = (T & 5) | ((T & 2) << 2) | ((T & 8) >> 2);
#pragma unroll
  for (int u = 0; u < 4; u++) {
    *(LDS3 u32x2*)(lds + L_PHI + T * 128 + (((2 * u + hh) ^ gs) << 3)) = u32x2{o[2 * u], o[2 * u + 1]};
    *(LDS3 u32x2*)(lds + L_PHI + T * 128 + (((8 + 2 * u + hh) ^ gs) << 3)) = u32x2{o[8 + 2 * u], o[9 + 2 * u]};
  }
}
DEV void v_phase(LDS3 char* lds, f32x16 accB, float t, const Stat& st, int g, int l, unsigned l16) {
  const bool pad = (l & 31) >= 28;
#pragma unroll
  for (int v = 0; v < 16; v++) accB[v] = pad ? 0.f : accB[v];
  accB = mfma(st.mfrag, cfrag(pad ? 0.f : t, l == 28 ? 0x3F80u : 0u), accB);
  *(LDS3 u32x4*)(lds + L_VFR + 2 * g * 1024 + l16) = pack8(accB, 0);
  *(LDS3 u32x4*)(lds + L_VFR + (2 * g + 1) * 1024 + l16) = pack8(accB, 8);
}
DEV f32x16 kv_phase(LDS3 char* lds, int g, int l, unsigned l16) {
  const int hh = l >> 5, q4 = (l & 15) >> 2, p4 = l & 3, blk = (l >> 4) & 1;
  f32x16 kv = {0};
  const int T0 = 4 * hh + q4;
  const unsigned gs0 = (T0 & 5) | ((T0 & 2) << 2);
  const unsigned a0 = L_PHI + T0 * 128 + (((8 * g + 4 * blk + p4) ^ gs0) << 3);
  const unsigned a1 = L_PHI + (T0 + 8) * 128 + (((8 * g + 4 * blk + p4) ^ gs0 ^ 2) << 3);
  u32x4 pa[8], vb[8];
#pragma unroll
  for (int s = 0; s < 8; s++) { pa[s] = ldtr(lds + a0 + s * 2048, lds + a1 + s * 2048); vb[s] = ld128(lds + L_VFR + s * 1024 + l16); }
#pragma unroll
  for (int s = 0; s < 8; s++) kv = mfma(pa[s], vb[s], kv);
  return kv;
}
template <int TG> DEV void kv_store(const f32x16& kv, unsigned vo, u32x4 sWS, unsigned so, unsigned tg) {
  const u32x4 a = pack8(kv, 0), b = pack8(kv, 8);
  so = __builtin_amdgcn_readfirstlane(so);
  vstore<TG, 0>(gran(a[0], a[1], tg), vo, sWS, so); vstore<TG, 1024>(gran(a[2], a[3], tg), vo, sWS, so);
  vstore<TG, 0>(gran(b[0], b[1], tg), vo, sWS, so + 32768); vstore<TG, 1024>(gran(b[2], b[3], tg), vo, sWS, so + 32768);
}
DEV void o_phase(const u32x4 (&kr)[8], const unsigned (&phq)[16], int hh, u32x4& o0, u32x4& o1) {
  f32x16 o = {0};
#pragma unroll
  for (int f = 0; f < 4; f++)
    o = mfma(u32x4{kr[2 * f][0], kr[2 * f][2], kr[2 * f + 1][0], kr[2 * f + 1][2]}, u32x4{phq[4 * f], phq[4 * f + 1], phq[4 * f + 2], phq[4 * f + 3]}, o);
  auto sw = __builtin_amdgcn_permlane32_swap(__float_as_uint(o[12]), __float_as_uint(o[12]), false, false);
  const float rc = __builtin_amdgcn_rcpf(__uint_as_float(sw[1]) + 1e-6f);
#pragma unroll
  for (int v = 0; v < 16; v++) { float t = o[v] * ((v >= 12 && hh) ? 0.f : rc); pin(t); o[v] = t; }
  o0 = pack8(o, 0); o1 = pack8(o, 8);
}

#define TS(k) do {} while (0)
#define TR(k) do {} while (0)
#define TEND() do {} while (0)

template <int CH>
DEV void bodyH(const u16* __restrict__ x, const u16* __restrict__ wqkv, const u16* __restrict__ wphi, const u16* __restrict__ wo, const u16* __restrict__ w1, const u16* __restrict__ b1,
               const u16* __restrict__ w2, u16* __restrict__ out, char* ws, LDS3 char* lds, const int w, const int r, const int c,
               const int R, const int nt) {
#define WV(t) VW[CH][t], t
  const int l = threadIdx.x & 63, lr = l & 31, hh = l >> 5;
  const int g = w & 3;
  const unsigned ldsb = (unsigned)(size_t)lds;
  TS(40); TR(42);
  asm volatile("; BODY %0" ::"n"(CH));
  const u32x4 sX = mksrd(x, 2048 * 896), sW1 = mksrd(w1, 448 * 3584), sWS = mksrd(ws, WS_END);
  const unsigned m1 = ldsb + w * 1024;
  const unsigned vX = (l >> 3) * 896 + (((l & 7) ^ (((w & 1) << 2) | (l >> 4))) << 4);
  const unsigned vW1 = (l >> 4) * 3584 + (((l & 15) ^ ((l >> 4) << 2)) << 4);
  const unsigned bX = (128 * r + 8 * w) * 896, bW1 = 4 * w * 3584 + c * 256;
  auto pageA = [&](auto KC, auto J) {
    constexpr int kc = KC, j = J, sl = (2 * kc) & 7;
    if constexpr (j < 2) glds<false, T_X + kc, sl * SLOT + j * 8192, j * 64 * 896>(m1, vX, sX, bX + kc * 128);
    else glds<false, T_X + kc, (sl + 1) * SLOT + (j - 2) * 8192, 32 * (j - 2) * 3584>(m1, vW1, sW1, bW1 + kc * (64 * 3584));
  };
#define IC(n) std::integral_constant<int, (n)>{}
  sfor<0, 4>([&](auto J) { pageA(IC(0), J); });
  const unsigned l16 = l * 16;
  u32x2 tagv;
  u32x4 xrow;
  unsigned b1v[2];
  {
    const u32x4 sB1 = mksrd(b1, 1792 * 2);
    const unsigned vb = lr * 2, sb = (128 * c + 64 * CH) * 2;
    asm volatile("buffer_load_dwordx2 %0, off, %1, 0 sc1 ; T%2" : "=v"(tagv) : "s"(sWS), "n"((int)T_TAG) : "memory");
    vload<false, T_TAG, 0>(xrow, l < 56 ? l16 : OOB, sX, (128 * r + 8 * c + w) * 896);
    asm volatile("buffer_load_ushort %0, %1, %2, %3 offen ; T%4" : "=v"(b1v[0]) : "v"(vb), "s"(sB1), "s"(sb), "n"((int)T_TAG) : "memory");
    asm volatile("buffer_load_ushort %0, %1, %2, %3 offen offset:64 ; T%4" : "=v"(b1v[1]) : "v"(vb), "s"(sB1), "s"(sb), "n"((int)T_TAG) : "memory");
  }
  sfor<0, 4>([&](auto J) { pageA(IC(1), J); });
  prefetch<T_WM, TW2 ? 7 : 15>(ldsb + L_PFD, w, l, x, wqkv, wo, w1, w2, out, ws);
  SB();
  const int q4 = (l & 15) >> 2, p4 = l & 3, blk = (l >> 4) & 1;
  const u32x4 sW2 = mksrd(w2, 1792 * 896), sWO = mksrd(wo, 448 * 896);
  const unsigned m2 = ldsb + w * 2048;
  const unsigned vW2 = (l >> 3) * 896 + (((l & 7) ^ (((l >> 4) & 1) << 2)) << 4);
  const unsigned bW2 = (128 * CH + 16 * g) * 896 + nt * 128, bWO = 56 * w * 896 + nt * 128;
  const u32x4 sQ = mksrd(wqkv, 448 * 2688);
  const unsigned vQ = (l >> 2) * 2688 + (l & 3) * 16, bQ = 16 * g * 2688 + ((56 * c) & ~15);
  constexpr int NA = CH ? 14 - XB : 14 + XB, NS = NA + 4;
  constexpr unsigned XM = 0x2AAA & ~((1u << (14 - 2 * XB)) - 1), AM = (CH ? 0x2AAA & ~XM : 0x1555 | XM) * 0x10001u;
  auto pageW = [&](auto P, auto J) {
    constexpr int p = P, j = J;
    constexpr unsigned km = ((p + 2) & 7) * SLOT + j * 1024;
    if constexpr (p < 14 && p < NA) glds<false, T_W + p, km, (8 * j + 256 * (p % 7) + (p < 7 ? 64 : 0)) * 896>(m2, vW2, sW2, bW2);
    else if constexpr (p < NA) glds<false, T_W + p, km, (8 * j + 256 * (p - XB - 7) + 128) * 896>(m2, vW2, sW2, bW2);
    else {
      constexpr int jj = p - NA;
      if constexpr ((jj & 1) && j == 1) glds<false, T_W + p, km, (28 * (jj >> 1) + 24) * 896>(m2, vW2 - (hh ? 4 * 896 : 0), sWO, bWO);
      else glds<false, T_W + p, km, (28 * (jj >> 1) + 16 * (jj & 1) + 8 * j) * 896>(m2, vW2, sWO, bWO);
    }
  };
  const unsigned vfl = l == 0 ? 0u : OOB;
  auto pageK = [&](auto J) { constexpr int j = J; glds<false, T_KV01, L_PHI + j * 8192, 64 * (2 * j + CH) * 2688 + 896>(m1, vQ, sQ, bQ); };
  auto pageV = [&](auto J) { constexpr int j = J, k = 2 * j + CH; glds<false, T_KV23, 6 * SLOT + j * 8192, (k < 7 ? 64 * k * 2688 + 1792 : 384 * 2688 + 896)>(m1, vQ, sQ, bQ); };
  auto pageQ = [&](auto J) { constexpr int j = J; glds<false, T_Q, j * 8192, 64 * (2 * j + CH) * 2688>(m1, vQ, sQ, bQ); };
  const u32x4 sel = mksel(l);

  unsigned xa[4], wa[4][2];
#pragma unroll
  for (int tt = 0; tt < 4; tt++) {
    const int s = (tt + g) & 3, row = 32 * g + lr;
    xa[tt] = row * 128 + (((2 * s + hh) ^ ((row >> 1) & 7)) << 4);
#pragma unroll
    for (int i = 0; i < 2; i++) wa[tt][i] = (16 * s + 8 * hh + q4) * 256 + ((((8 * CH + 4 * i) ^ (q4 << 2)) + 2 * blk + (p4 >> 1)) << 4) + 8 * (p4 & 1);
  }

#pragma unroll
  for (int tt = 0; tt < 4; tt++) pin(xa[tt], wa[tt][0], wa[tt][1]);
  u32x4 xf[28], af[28][2];
  f32x16 acc[2] = {{0}, {0}};
  f32x4 cs0 = {0}, cs1 = {0};
  u32x4 sg[1];
  u64 tag;
  u32x2 fl;
  auto RA = [&](auto S) {
    constexpr int s = S, kc = s >> 2, tt = s & 3;
    constexpr unsigned sx = ((2 * kc) & 3) * SLOT, sw = sx + SLOT;
    xf[s] = ld128(lds + xa[tt] + sx);
    af[s][0] = ldtr(lds + wa[tt][0] + sw, lds + wa[tt][0] + sw + 1024);
    af[s][1] = ldtr(lds + wa[tt][1] + sw, lds + wa[tt][1] + sw + 1024);
  };
  auto DA = [&](auto S) {
    constexpr int s = S;
    if constexpr ((s & 3) == 0) { rowsum(cs0, sel, af[s][0]); rowsum(cs1, sel, af[s][1]); }
  };
  const unsigned vST = lr * 16, oST = __builtin_amdgcn_readfirstlane(WS_ST + (128 * r + 32 * g) * 16);
  sfor<0, 7>([&](auto KC) {
    constexpr int kc = KC;
    if constexpr (kc == 0) barw<WV(T_TAG)>();
    else barw<WV(T_X + kc)>();
    need<T_X + kc>();
    TS(kc);
    if constexpr (kc == 0) { pin(tagv, b1v[0], b1v[1], xrow); tag = mktag(tagv); }
    if constexpr (kc == 2 || kc == 4 || kc == 6) {
#pragma unroll
      for (int tt = 0; tt < 4; tt++) {
        xa[tt] ^= 0x10000u; wa[tt][0] ^= 0x10000u; wa[tt][1] ^= 0x10000u;
        asm volatile("" : "+v"(xa[tt]), "+v"(wa[tt][0]), "+v"(wa[tt][1]));
      }
    }
    if constexpr (kc == 1) {
      if (w == 7) {
        const u32x4 sP = mksrd(wphi, 16 * 1568);
        glds<false, 99, L_WF, 0>(ldsb, c * 1568 + l16, sP, 0); glds<false, 99, L_WF + 1024, 0>(ldsb, c * 1568 + 1024 + l16, sP, 0);
      }
    }
    if constexpr (kc == 6) vload<true, T_ST, 0>(sg[0], vST, sWS, oST);
    sfor<0, 4>([&](auto TT) {
      constexpr int tt = TT, s = 4 * kc + tt;
      RA(IC(s));
      SB();
      if constexpr (s >= 2) mf(acc[0], af[s - 2][0], xf[s - 2]);
      SB();
      if constexpr (kc == 0) { pageA(IC(2 + (tt >> 1)), IC(2 * (tt & 1))); pageA(IC(2 + (tt >> 1)), IC(2 * (tt & 1) + 1)); }
      else if constexpr (kc <= 3) pageA(IC(kc + 3), TT);
      else if constexpr (kc == 4) pageV(TT);
      else if constexpr (kc == 5) { if constexpr (tt < 4 - CH) pageQ(TT); }
      else pageW(IC(tt >> 1), IC(tt & 1));
      if constexpr (KE == 0 && kc == 0 && tt == 3) { pageK(IC(0)); pageK(IC(1)); pageK(IC(2)); }
      if constexpr (KE == 1 && kc >= 1 && kc <= 3 && tt == 3) pageK(IC(kc ? kc - 1 : 0));
      if constexpr (KE == 2 && kc == 3 && tt >= 1) pageK(IC(tt ? tt - 1 : 0));
      SB();
      if constexpr (s >= 2) { mf(acc[1], af[s - 2][1], xf[s - 2]); SB(); DA(IC(s - 2)); }
      SB();
    });
    if constexpr (TW2 && kc == TW2) prefetch<T_WM, 8>(ldsb + L_PFD, w, l, x, wqkv, wo, w1, w2, out, ws);
    if constexpr (kc == TS1) warm<T_WM>(ldsb + L_PFD, own_lines(CH, g, l, r, c), sWS, 0);
    if constexpr (kc == TA1) warm<T_WM>(ldsb + L_PFD, l * 64, sWS, __builtin_amdgcn_readfirstlane(WS_ACT + ((4 * r + g) * 144 + 8 * c + 4 * CH) * 1024));
    if constexpr (kc == 0) { vstore<T_FL, 0>(row_stats(xrow, l, (unsigned)tag), vfl, sWS, WS_ST + (128 * r + 8 * c + w) * 16); SB(); }
  });
  *(LDS3 float*)(lds + L_CS + (2 * CH) * 1024 + l16 + g * 4) = cs0[0];
  *(LDS3 float*)(lds + L_CS + (2 * CH + 1) * 1024 + l16 + g * 4) = cs1[0];

  const unsigned ftr = (8 * hh + q4) * 64 + blk * 32 + p4 * 8 + (c & 1) * 8;
  unsigned fa[4];
#pragma unroll
  for (int tt = 0; tt < 4; tt++) { fa[tt] = ((tt + g) & 3) * 1024 + ftr + 6 * SLOT; pin(fa[tt]); }
  barw<WV(T_ST)>();
  need<T_KV01>(); need<T_KV23>(); need<T_ST>();
  TS(7);
  if constexpr (TS1 == 7) warm<T_WM>(ldsb + L_PFD, own_lines(CH, g, l, r, c), sWS, 0);
  if constexpr (OT && CH == 0) warm<T_WM>(ldsb + L_PFD, g < 2 ? OOB : (32 * (g - 2) + (l >> 1)) * 896 + (l & 1) * 64, mksrd(out, 2048 * 896), 64 * R * 896 + nt * 128);
  if constexpr (TA1 == 7 && CH == 0) warm<T_WM>(ldsb + L_PFD, l * 64, sWS, __builtin_amdgcn_readfirstlane(WS_ACT + ((4 * r + g) * 144 + 8 * c + 4 * CH) * 1024));
  if constexpr (PFL) {
    const int bj = blockIdx.x >> 3, u = (blockIdx.x & 1) ? w * 24 + bj - 8 : w * 32 + bj, bk = u / 7;
    if (u < 161) {
      const unsigned o = (bk < 14 ? 128 * bk : bk < 16 ? 128 * bk - 192 : 64 * bk - 1024) * 896 + (u - 7 * bk) * 8192;
      const u32x4 sP = mksrd(bk < 16 ? w2 : wo, 1792 * 896);
      warm<99>(ldsb + L_PFD, l * 128, sP, o);
      if constexpr (PFL == 2) warm<99>(ldsb + L_PFD, l * 128 + 64, sP, o);
    }
  }
  u32x4 f[28];
  f32x16 accB = {0};
  auto RB = [&](auto S) { constexpr int s = S; constexpr int o = CH ? (s >> 2) * 4096 : s < 24 ? 2 * SLOT + (s >> 2) * 4096 : 7 * 4096; f[s] = ldtr(lds + fa[s & 3] + o, lds + fa[s & 3] + o + 256); };
  f32x4 csB = {0};
  auto MB = [&](auto S) {
    constexpr int s = S;
    if constexpr (CH == 0) mf(accB, f[s], xf[s]); else mf(accB, xf[s], f[s]);
    rowsum(csB, sel, f[s]);
  };
  const f32x4 ct0 = *(LDS3 f32x4*)(lds + L_CS + (2 * CH) * 1024 + l16), ct1 = *(LDS3 f32x4*)(lds + L_CS + (2 * CH + 1) * 1024 + l16);
  if constexpr (CH == 0) { RB(IC(0)); RB(IC(1)); RB(IC(2)); } else __builtin_amdgcn_s_setprio(2);
  SB();
  mf(acc[0], af[26][0], xf[26]);
  mf(acc[1], af[26][1], xf[26]);
  mf(acc[0], af[27][0], xf[27]);
  mf(acc[1], af[27][1], xf[27]);
  SB();
  pin(sg[0]);
  if (__builtin_expect(stale(sg, (unsigned)tag), 0)) refetch<1, 0>(sg, vST, sWS, oST, (unsigned)tag);
  const Stat st = stats(sg[0]);
  mf(acc[0], cfrag(sum4(ct0), hh ? 0u : b1v[0]), st.mfrag);
  mf(acc[1], cfrag(sum4(ct1), hh ? 0u : b1v[1]), st.mfrag);
  SB();
  SB();
  TS(32);
  const unsigned actv = (4 * r + g) * 144 * 1024 + l16;
  float mt[8], mu[8], md[8];
  auto MI = [&](auto Q) {
    constexpr int q = Q, G = q >> 2, ck = q & 3, i = G >> 1, o = 8 * (G & 1);
    if constexpr (ck == 0) {
      sfor<0, 8>([&](auto V) { mt[V] = acc[i][o + V] * st.rl; pin(mt[V]); });
      sfor<0, 4>([&](auto V) { mu[V] = __builtin_amdgcn_exp2f(mt[V]); pin(mu[V]); });
    } else if constexpr (ck == 1) {
      sfor<4, 8>([&](auto V) { mu[V] = __builtin_amdgcn_exp2f(mt[V]); pin(mu[V]); });
      sfor<0, 8>([&](auto V) { md[V] = mu[V] + 2.f; pin(md[V]); });
      sfor<0, 4>([&](auto V) { md[V] = __builtin_fmaf(mu[V], md[V], 2.f); pin(md[V]); });
    } else if constexpr (ck == 2) {
      sfor<4, 8>([&](auto V) { md[V] = __builtin_fmaf(mu[V], md[V], 2.f); pin(md[V]); });
      sfor<0, 8>([&](auto V) { md[V] = __builtin_amdgcn_rcpf(md[V]); pin(md[V]); });
    } else {
      sfor<0, 8>([&](auto V) { md[V] = __builtin_fmaf(md[V], -1.38629436111989f, 0.693147180559945f); pin(md[V]); });
      sfor<0, 8>([&](auto V) { md[V] = md[V] * mt[V]; pin(md[V]); });
      vstore<T_AST, G * 1024>(u32x4{pk2(md[0], md[1]), pk2(md[2], md[3]), pk2(md[4], md[5]), pk2(md[6], md[7])}, actv, sWS, WS_ACT + (8 * c + 4 * CH) * 1024);
    }
  };
  const u32x2 tagw = __builtin_bit_cast(u32x2, tag);
  auto AFLAG = [&]() { vwait<WV(T_AST)>(); fstore(tagw, vfl, sWS, WS_AF + (((4 * r + g) * 2 + CH) * 16 + c) * FS); };
  __builtin_amdgcn_s_setprio(1 - CH);
  if constexpr (CH == 1) {
    sfor<0, 16>([&](auto Q) { constexpr int q = Q; if constexpr (q >= 13) RB(IC(q - 13)); SB(); MI(Q); SB(); });
    TS(35);
    bar<63>();
    __builtin_amdgcn_s_setprio(1);
  }
  sfor<3, 31>([&](auto N) {
    constexpr int n = N, s = n - 3;
    if constexpr (n < 28) RB(N);
    SB();
    MB(IC(s)); SB();
    if constexpr (s % 4 == 1 && s < 16) pageW(IC(2 + (s >> 3)), IC((s >> 2) & 1));
    SB();
  });
  TS(37);
  __builtin_amdgcn_s_setprio(0);
  if constexpr (CH == 0) bar<63>();
  TS(8);
  if constexpr (CH == 0) k_phase(lds, accB, csB[0], st, g, l);
  else { v_phase(lds, accB, csB[0], st, g, l, l16); SB(); TS(33); AFLAG(); }
  TS(38);
  bar<63>();
  need<T_Q>();
  TS(9);
  unsigned phq[16];
  const unsigned oAF = WS_AF + 4 * R * 16 * FS, vAF = l < 32 ? (l + (l & 16)) * FS : OOB;
  if constexpr (CH == 0) {
    if (g < 2) {
      __builtin_amdgcn_s_setprio(2);
      kv_store<99>(kv_phase(lds, g, l, l16), l16, sWS, WS_KVP + ((c * 4 + 2 * g) * 16 + r) * 2048, (unsigned)tag);
      __builtin_amdgcn_s_setprio(0);
    }
    TS(33);
    sfor<0, 16>([&](auto Q) { MI(Q); SB(); });
    TS(34);
    sfor<0, 4>([&](auto J) { constexpr int j = J; pageW(IC(4 + (j >> 1)), IC(j & 1)); });
    AFLAG();
  } else {
    unsigned qa[4];
#pragma unroll
    for (int tt = 0; tt < 4; tt++) qa[tt] = ((tt + g) & 3) * 1024 + ftr;
    u32x4 fq[28];
    f32x16 accQ = {0};
    f32x4 csQ = {0};
    sfor<0, 31>([&](auto N) {
      constexpr int n = N, s = n - 3;
      if constexpr (n < 28) fq[n] = ldtr(lds + qa[n & 3] + (n >> 2) * 4096, lds + qa[n & 3] + (n >> 2) * 4096 + 256);
      SB();
      if constexpr (s >= 0) { mf(accQ, fq[s], xf[s]); rowsum(csQ, sel, fq[s]); }
      SB();
      if constexpr (s % 4 == 1 && s < 16) pageW(IC(4 + (s >> 3)), IC((s >> 2) & 1));
      SB();
    });
    TS(34);
    if constexpr (EF) fload<T_RF>(fl, g == 2 ? vAF : OOB, sWS, oAF + 16 * FS);
    dual_softmax(logits(lds, accQ, csQ[0], st, l), st.rl, 1.f, hh != 0, phq);
#pragma unroll
    for (int i = 0; i < 16; i++) pin(phq[i]);
  }
  TS(39);
  if constexpr (CH == 1 && EF) { vwait<WV(T_RF)>(fl); check(fl, vAF, sWS, oAF + 16 * FS, tag, g == 2 ? 0x3FFF3FFF : 0); }
  if constexpr (CH == 1 && !EF) poll(vAF, sWS, oAF + 16 * FS, tag, g == 2 ? 0x3FFF3FFF : 0);
  barw<WV(T_W + 1)>();
  TS(10);
  constexpr int LAX = CH ? LA1 : LA;
  u32x4 al[NS][2], aw[NS][2], kr[8], xr;
  const unsigned actr = (2 * R * 144 + 8 * CH + g) * 1024 + l16;
  const unsigned vAF0 = l < 32 && ((AM >> (l & 31)) & 1) ? vAF : OOB;
  auto LACT = [&](auto P) {
    constexpr int p = P;
    if constexpr (p < NA) {
      constexpr unsigned o = p < 14 ? (p % 7) * 16384 + (p < 7 ? 4096 : 0) : (p - XB - 7) * 16384 + 8192;
      vload<true, T_ACT + p, 0>(al[p][0], actr, sWS, WS_ACT + o);
      vload<true, T_ACT + p, 0>(al[p][1], actr, sWS, WS_ACT + o + 144 * 1024);
    }
  };
  if constexpr (CH == 1 && PR) __builtin_amdgcn_s_setprio(3);
  sfor<0, LAX>([&](auto P) { LACT(P); });
  sfor<0, 4>([&](auto J) { constexpr int j = J; pageW(IC(6 + (j >> 1)), IC(j & 1)); });
  TS(11);
  unsigned ua[2][2];
#pragma unroll
  for (int i = 0; i < 2; i++) {
    ua[0][i] = w * 2048 + (4 * hh + q4) * 128 + ((((4 * i) ^ ((q4 >> 1) << 2)) + 2 * blk + (p4 >> 1)) << 4) + 8 * (p4 & 1);
    ua[1][i] = ua[0][i] + 65536;
  }
  auto RW = [&](auto P) {
    constexpr int p = P, sl = (p + 2) & 7;
    need<T_W + p>();
#pragma unroll
    for (int i = 0; i < 2; i++) aw[p][i] = ldtr(lds + ua[sl >> 2][i] + (sl & 3) * SLOT, lds + ua[sl >> 2][i] + (sl & 3) * SLOT + 1024);
  };
  f32x16 oa[2][2] = {{{0}, {0}}, {{0}, {0}}};
  const unsigned oOG = __builtin_amdgcn_readfirstlane(WS_OG + (32 * R + 2 * w) * 3072);
  u32x4 og[12];
  RW(IC(0));
  sfor<0, TL ? NA + 1 : NS>([&](auto P) {
    constexpr int p = P;
    if constexpr (p < NS - 1) RW(IC(p + 1));
    if constexpr (TL && p == NA) { RW(IC(p + 2)); RW(IC(p + 3)); }
    if constexpr (p + 8 < NS) {
      asm volatile("s_waitcnt lgkmcnt(4)" ::: "memory");
      pageW(IC(p + 8), IC(0)); pageW(IC(p + 8), IC(1));
    }
    if constexpr (CH == 1 && p == KP) sfor<0, 8>([&](auto J) { constexpr int j = J; vload<true, T_KVR, (j & 3) * 1024>(kr[j], l16, sWS, WS_KVR + c * 8192 + (j >> 2) * 4096); });
    if constexpr (p + LAX == 6) fload<T_AF>(fl, vAF0, sWS, oAF);
    if constexpr (p + LAX == 7) { vwait<WV(T_AF)>(fl); check(fl, vAF0, sWS, oAF, tag, AM); TS(36); }
    if constexpr (p + LAX < NA) LACT(IC(p + LAX));
    if constexpr (p == NA + (CH ? OP1 : OP) - 14) {
      o_load<T_OF>(og, l16, sWS, oOG);
      vload<false, T_XR, 0>(xr, (threadIdx.x >> 3) * 896 + (threadIdx.x & 7) * 16, sX, 64 * R * 896 + nt * 128);
    }
    if constexpr (p < NA) vwait<WV(T_ACT + p)>(al[p][0], al[p][1]);
    if constexpr (p == NA) {
      vwait<WV(T_OF)>(og[0], og[1], og[2], og[3]);
      pin(og[4], og[5], og[6], og[7], og[8], og[9], og[10], og[11]);
      if (__builtin_expect(o_stale(og, (unsigned)tag), 0)) {
        int it = SPIN_MAX;
        do {
          poll(l < 4 ? (l >> 1) * 49152 + (l & 1) * 3072 + 2552 : OOB, sWS, oOG, (tag & 0xffffffffull) * 0x100000001ull, 0xF);
          o_load<99>(og, l16, sWS, oOG);
          asm volatile("s_waitcnt vmcnt(0)" ::: "memory");
          pin(og[0], og[1], og[2], og[3], og[4], og[5], og[6], og[7], og[8], og[9], og[10], og[11]);
        } while (o_stale(og, (unsigned)tag) && --it > 0);
      }
#pragma unroll
      for (int i = 0; i < 4; i++) {
        const u32x4 a = og[3 * i], b = og[3 * i + 1], d = og[3 * i + 2];
        al[NA + 2 * (i & 1)][i >> 1] = u32x4{a[0], a[1], a[2], b[0]}; al[NA + 1 + 2 * (i & 1)][i >> 1] = u32x4{b[1], b[2], d[0], d[1]};
      }
    }
    TS(p < NA ? (p < 14 ? 12 + p : 44 + p - 14) : 26 + p - NA);
    if constexpr (!TL || p < NA) {
#pragma unroll
      for (int t = 0; t < 2; t++)
#pragma unroll
        for (int i = 0; i < 2; i++) mf(oa[i][t], aw[p][i], al[p][t]);
    }
    SB();
    if constexpr (CH == 1 && p == KP + 1) {
      vwait<WV(T_KVR)>(kr[0], kr[1], kr[2], kr[3]);
      pin(kr[4], kr[5], kr[6], kr[7]);
      if (__builtin_expect(stale(kr, (unsigned)tag), 0)) refetch<8, 1024>(kr, l16, sWS, WS_KVR + c * 8192, (unsigned)tag);
      TS(31);
      u32x4 o0, o1;
      o_phase(kr, phq, hh, o0, o1);
      o_store<T_OST>(o0, o1, l16, sWS, WS_OG + ((4 * r + g) * 16 + c) * 3072, (unsigned)tag);
      if constexpr (PR == 1) __builtin_amdgcn_s_setprio(0);
    }
    SB();
  });
  {
    const unsigned pw = w * 2048 + ((hh * 32 + (lr ^ (hh * 8))) << 4);
    auto put = [&](int ck, int t) {
      const f32x16& a = oa[ck >> 2][t];
      const int u = ck & 3;
      *(LDS3 f32x4*)(lds + ((pw ^ (ck << 4)) + (ck >> 2) * 65536) + ((ck & 3) * SLOT + t * 1024)) = f32x4{a[4 * u], a[4 * u + 1], a[4 * u + 2], a[4 * u + 3]};
    };
    if constexpr (TL) {
#pragma unroll
      for (int q = 0; q < 4; q++) { mf(oa[0][0], aw[NA + q][0], al[NA + q][0]); mf(oa[0][1], aw[NA + q][0], al[NA + q][1]); }
      SB();
#pragma unroll
      for (int q = 0; q < 4; q++) {
        mf(oa[1][0], aw[NA + q][1], al[NA + q][0]); mf(oa[1][1], aw[NA + q][1], al[NA + q][1]);
        SB();
        if (q == 1) { put(0, 0); put(1, 0); put(2, 0); put(3, 0); }
        if (q == 2) { put(0, 1); put(1, 1); put(2, 1); put(3, 1); }
        SB();
      }
      TS(29);
#pragma unroll
      for (int t = 0; t < 2; t++)
#pragma unroll
        for (int ck = 4; ck < 8; ck++) put(ck, t);
    } else {
#pragma unroll
      for (int ck = 0; ck < 8; ck++)
#pragma unroll
        for (int t = 0; t < 2; t++) put(ck, t);
    }
  }
  barw<WV(T_XR)>();
  TS(30);
  asm volatile("" : "+v"(xr));
  {
    const int a = l >> 3, ck = l & 7, hs = (a >> 1) & 1, lr2 = 8 * (w & 3) + a;
    const unsigned base = ck * SLOT + (w >> 2) * 1024;
    const unsigned r0 = base + (((hs * 32 + (lr2 ^ (hs * 8))) ^ ck) << 4), r1 = base + ((((hs ^ 1) * 32 + (lr2 ^ ((hs ^ 1) * 8))) ^ ck) << 4);
    f32x4 s0[8], s1[8];
#pragma unroll
    for (int q = 0; q < 8; q++) { s0[q] = *(LDS3 f32x4*)(lds + r0 + q * 2048); s1[q] = *(LDS3 f32x4*)(lds + r1 + q * 2048); }
    const f32x4 a0 = ((s0[0] + s0[1]) + (s0[2] + s0[3])) + ((s0[4] + s0[5]) + (s0[6] + s0[7]));
    const f32x4 a1 = ((s1[0] + s1[1]) + (s1[2] + s1[3])) + ((s1[4] + s1[5]) + (s1[6] + s1[7]));
    const f32x4 lo = hs ? a1 : a0, hi = hs ? a0 : a1;
    u32x4 o;
    o[0] = pk2(lo[0] + bflo(xr[0]), lo[1] + bfhi(xr[0])); o[1] = pk2(lo[2] + bflo(xr[1]), lo[3] + bfhi(xr[1]));
    o[2] = pk2(hi[0] + bflo(xr[2]), hi[1] + bfhi(xr[2])); o[3] = pk2(hi[2] + bflo(xr[3]), hi[3] + bfhi(xr[3]));
    ostore<T_FL>(o, (threadIdx.x >> 3) * 896 + (threadIdx.x & 7) * 16, mksrd(out, 2048 * 896), 64 * R * 896 + nt * 128);
  }
  TEND();
#undef WV
}

template <int CH>
DEV void bodyA(const u16* __restrict__ x, const u16* __restrict__ wqkv, const u16* __restrict__ wphi, const u16* wo, const u16* w1, const u16* w2, const u16* out, char* ws, LDS3 char* lds,
               const int w, const int r, const int c) {
  const int l = threadIdx.x & 63, lr = l & 31, hh = l >> 5;
  const int g = w & 3, a = 2 * r + (c - 14);
  const unsigned ldsb = (unsigned)(size_t)lds;
  TS(40); TR(42);
  asm volatile("; BODY %0" ::"n"(CH + 2));
  const u32x4 sX = mksrd(x, 2048 * 896), sWS = mksrd(ws, WS_END);
  const unsigned l16 = l * 16, m1 = ldsb + w * 1024;
  const unsigned vX = (l >> 3) * 896 + (((l & 7) ^ (((w & 1) << 2) | (l >> 4))) << 4);
  const unsigned bX = (128 * r + 8 * w) * 896;
  const unsigned vfl = l == 0 ? 0u : OOB;
  u32x2 tagv;
  u32x4 xrow;
  asm volatile("buffer_load_dwordx2 %0, off, %1, 0 sc1" : "=v"(tagv) : "s"(sWS) : "memory");
  vload<false, 99, 0>(xrow, l < 56 ? l16 : OOB, sX, (128 * r + 8 * c + w) * 896);
  sfor<0, 14>([&](auto J) { constexpr int j = J; glds<false, 99, j * 8192, (j & 1) * 64 * 896 + (j >> 1) * 128>(m1, vX, sX, bX); });
  const u32x4 sQ = mksrd(wqkv, 448 * 2688);
  {
    const u32x4 sP = mksrd(wphi, 16 * 1568);
    glds<false, 99, L_WF, 0>(ldsb + (w & 1) * 1024, c * 1568 + (w & 1) * 1024 + l16, sP, 0);
  }
  prefetch<99, 15>(ldsb + L_PFD, w, l, x, wqkv, wo, w1, w2, out, ws);
  asm volatile("s_waitcnt vmcnt(16)" : "+v"(tagv), "+v"(xrow)::"memory");
  const u64 tag = mktag(tagv);
  const unsigned tg = (unsigned)tag;
  const u32x2 tagw = __builtin_bit_cast(u32x2, tag);
  TS(1);
  vstore<99, 0>(row_stats(xrow, l, tg), vfl, sWS, WS_ST + (128 * r + 8 * c + w) * 16);
  bar<3>();
  TS(2);
  u32x4 xf[28];
  {
    unsigned xa[2][4];
#pragma unroll
    for (int tt = 0; tt < 4; tt++) {
      const int s = (tt + g) & 3, row = 32 * g + lr;
      xa[0][tt] = row * 128 + (((2 * s + hh) ^ ((row >> 1) & 7)) << 4); xa[1][tt] = xa[0][tt] + 65536;
    }
#pragma unroll
    for (int s = 0; s < 28; s++) xf[s] = ld128(lds + xa[s >> 4][s & 3] + ((s >> 2) & 3) * SLOT);
  }
  bar<63>();
  TS(3);
  const unsigned vQ = (l >> 2) * 2688 + (l & 3) * 16, bQ = 16 * g * 2688 + ((56 * c) & ~15);
  const unsigned vST = lr * 16, oST = __builtin_amdgcn_readfirstlane(WS_ST + (128 * r + 32 * g) * 16);
  const unsigned ftr = (8 * hh + ((l & 15) >> 2)) * 64 + ((l >> 4) & 1) * 32 + (l & 3) * 8 + (c & 1) * 8;
  const u32x4 sel = mksel(l);
  u32x4 sg[1];
  sfor<0, 7>([&](auto J) { constexpr int j = J; glds<false, 99, j * 8192, 64 * j * 2688 + (1 + CH) * 896>(m1, vQ, sQ, bQ); });
  vload<true, 99, 0>(sg[0], vST, sWS, oST);
  sfor<0, 4 - CH>([&](auto J) { constexpr int j = J; glds<false, 99, 4 * SLOT + j * 8192, 64 * (2 * j + CH) * 2688>(m1, vQ, sQ, bQ); });
  bar<4 - CH>();
  TS(7);
  if constexpr (TS1 < 8) {
    warm<99>(ldsb + L_PFD, own_lines(CH, g, l, r, c), sWS, 0);
    warm<99>(ldsb + L_PFD, l < 8 ? (l >> 2) * 1024 + (l & 3) * 64 : OOB, sWS, __builtin_amdgcn_readfirstlane(WS_KVR + (2 * a + CH) * 2048 + g * 256));
  }
  pin(sg[0]);
  if (__builtin_expect(stale(sg, tg), 0)) refetch<1, 0>(sg, vST, sWS, oST, tg);
  const Stat st = stats(sg[0]);
  f32x4 csB = {0}, csQ = {0};
  f32x16 accB = {0};
  {
    unsigned fa[4];
#pragma unroll
    for (int tt = 0; tt < 4; tt++) fa[tt] = ((tt + g) & 3) * 1024 + CH * 4096 + ftr;
    u32x4 f[28];
    if constexpr (CH == 0) __builtin_amdgcn_s_setprio(1);
    sfor<0, 31>([&](auto N) {
      constexpr int n = N, s = n - 3;
      if constexpr (n < 28) f[n] = ldtr(lds + fa[n & 3] + (n >> 2) * 8192, lds + fa[n & 3] + (n >> 2) * 8192 + 256);
      SB();
      if constexpr (s >= 0) { if constexpr (CH == 0) mf(accB, f[s], xf[s]); else mf(accB, xf[s], f[s]); rowsum(csB, sel, f[s]); }
      if constexpr (CH == 1 && n == 17) bar<(TS1 < 8 ? 2 : 0)>();
      SB();
    });
    if constexpr (CH == 0) { __builtin_amdgcn_s_setprio(0); bar<(TS1 < 8 ? 2 : 0)>(); }
  }
  TS(9);
  f32x16 accQ = {0};
  if constexpr (CH == 0) k_phase(lds, accB, csB[0], st, g, l);
  else {
    unsigned qa[4];
#pragma unroll
    for (int tt = 0; tt < 4; tt++) qa[tt] = 4 * SLOT + ((tt + g) & 3) * 1024 + ftr;
    u32x4 fq[28];
    v_phase(lds, accB, csB[0], st, g, l, l16);
    SB();
    sfor<0, 31>([&](auto N) {
      constexpr int n = N, s = n - 3;
      if constexpr (n < 28) fq[n] = ldtr(lds + qa[n & 3] + (n >> 2) * 4096, lds + qa[n & 3] + (n >> 2) * 4096 + 256);
      SB();
      if constexpr (s >= 0) { mf(accQ, fq[s], xf[s]); rowsum(csQ, sel, fq[s]); }
      SB();
    });
  }
  TS(8);
  bar<63>();
  TS(10);
  unsigned phq[16];
  if constexpr (CH == 0) {
    if (g < 2) kv_store<99>(kv_phase(lds, g, l, l16), l16, sWS, WS_KVP + ((c * 4 + 2 * g) * 16 + r) * 2048, tg);
  } else {
    dual_softmax(logits(lds, accQ, csQ[0], st, l), st.rl, 1.f, hh != 0, phq);
#pragma unroll
    for (int i = 0; i < 16; i++) pin(phq[i]);
  }
  TS(11);
  {
    u32x4 t[8];
    const unsigned vo = (l >> 4) * 1024 + (l & 15) * 16;
    refetch<8, 4096>(t, vo, sWS, WS_KVP + (2 * a + CH) * 32768 + g * 256, tg);
    TS(12);
    unsigned o[2];
#pragma unroll
    for (int i = 0; i < 2; i++) {
      float lo[4], hi[4];
#pragma unroll
      for (int q = 0; q < 4; q++) { lo[q] = bflo(t[2 * q][2 * i]) + bflo(t[2 * q + 1][2 * i]); hi[q] = bfhi(t[2 * q][2 * i]) + bfhi(t[2 * q + 1][2 * i]); }
      o[i] = pk2(xhalf_add((lo[0] + lo[1]) + (lo[2] + lo[3])), xhalf_add((hi[0] + hi[1]) + (hi[2] + hi[3])));
    }
    vstore<99, 0>(gran(o[0], o[1], tg), l < 32 ? vo : OOB, sWS, WS_KVR + (2 * a + CH) * 2048 + g * 256);
    TS(13);
  }
  if constexpr (CH == 1) {
    u32x4 kr[8], o0, o1;
    refetch<8, 1024>(kr, l16, sWS, WS_KVR + c * 8192, tg);
    TS(14);
    o_phase(kr, phq, hh, o0, o1);
    o_store<99>(o0, o1, l16, sWS, WS_OG + ((4 * r + g) * 16 + c) * 3072, tg);
    TS(15);
  } else if (a == 31 && w == 0) {
    u32x4 t[8];
#pragma unroll 1
    for (int i = 0; i < 4; i++) refetch<8, 1024>(t, l16, sWS, WS_ST + i * 8192, tg);
    fstore(tagw, vfl, sWS, 0);
  }
  TEND();
}

__global__ __launch_bounds__(512, 2) void fused_kernel(const u16* __restrict__ x, const u16* __restrict__ wqkv, const u16* __restrict__ wphi,
                                                       const u16* __restrict__ wo, const u16* __restrict__ w1, const u16* __restrict__ b1,
                                                       const u16* __restrict__ w2, u16* __restrict__ out, char* ws) {
  __shared__ __attribute__((aligned(16))) char lds_[LDS_BYTES];
  LDS3 char* lds = (LDS3 char*)lds_;
  const int w = __builtin_amdgcn_readfirstlane(threadIdx.x >> 6);
  const int b = blockIdx.x, xcd = b & 7, j = b >> 3, p = xcd >> 1;
  const bool odd = xcd & 1;
  if (!odd || j >= 8) {
    const int m = j - 8;
    const int r = 4 * p + (odd ? m / 6 : j >> 3), c = odd ? 8 + m % 6 : j & 7;
    const int t = odd ? 32 + m : j;
    const int R = MAPT ? 8 * p + t / 7 : 8 * p + (odd ? m / 3 : j >> 2), nt = MAPT ? t % 7 : odd ? 4 + m % 3 : j & 3;
    if (w < 4) bodyH<0>(x, wqkv, wphi, wo, w1, b1, w2, out, ws, lds, w, r, c, R, nt);
    else bodyH<1>(x, wqkv, wphi, wo, w1, b1, w2, out, ws, lds, w, r, c, R, nt);
  } else {
    const int r = 4 * p + (j >> 1), c = 14 + (j & 1);
    if (w < 4) bodyA<0>(x, wqkv, wphi, wo, w1, w2, out, ws, lds, w, r, c);
    else bodyA<1>(x, wqkv, wphi, wo, w1, w2, out, ws, lds, w, r, c);
  }
}

extern "C" void kernel_launch(void* const* d_in, const int* in_sizes, int n_in, void* d_out, int out_size, void* d_ws, size_t ws_size,
                              hipStream_t stream) {
  fused_kernel<<<256, 512, 0, stream>>>((const u16*)d_in[0], (const u16*)d_in[1], (const u16*)d_in[2], (const u16*)d_in[3], (const u16*)d_in[5],
                                        (const u16*)d_in[6], (const u16*)d_in[7], (u16*)d_out,
                                        (char*)d_ws + (WOFF == 1 ? (ws_size - WS_END) & ~(size_t)0x1FFFFF : WOFF == 2 ? (ws_size / 2) & ~(size_t)0x1FFFFF : 0));
}
```
